```python
import torch

def _softmax(x, axis=-1):
    return torch.softmax(x, dim=axis)
def _gelu(x):
    return torch.nn.functional.gelu(x, approximate="tanh")
def _silu(x):
    return torch.nn.functional.silu(x)
def _sigmoid(x):
    return torch.sigmoid(x)
def _layernorm(x, axis=-1, eps=1e-5):
    m = x.mean(dim=axis, keepdim=True)
    v = x.var(dim=axis, keepdim=True, unbiased=False)
    return (x - m) / torch.sqrt(v + eps)
def _scatter_add(tgt, idx, src, axis=0):
    return tgt.clone().index_add_(axis, idx, src)
def _segment_sum(src, seg, n):
    out = torch.zeros((n,) + src.shape[1:], dtype=src.dtype)
    return out.index_add_(0, seg, src)
def _pad(x, pw):
    flat = tuple(v for pair in reversed(pw) for v in pair)
    return torch.nn.functional.pad(x, flat)

kernel_name = 'sequential_preLN__mha_block__mixer_token_mlp__prompt'

def setup_inputs():

    _w = (lambda a: (lambda t: t.to(torch.bfloat16) if t.is_floating_point() else t)(torch.as_tensor(a)))
    def _A_setup():
        B, S, D, H = 1, 2048, 640, 32
        return {
            "x": _w((lambda *s: torch.randn(*s, dtype=torch.bfloat16) * 0.05)(B, S, D)),
            "wqkv": _w((lambda *s: torch.randn(*s, dtype=torch.bfloat16) * 0.05)(D, 3 * D)),
            "wo": _w((lambda *s: torch.randn(*s, dtype=torch.bfloat16) * 0.05)(D, D)),
        }
    _A_in = _A_setup()

    def _B_setup():
        B, S, D, F = 1, 2048, 640, 1536
        return {
            "x": _w((lambda *s: torch.randn(*s, dtype=torch.bfloat16) * 0.05)(B, S, D)),
            "w1": _w((lambda *s: torch.randn(*s, dtype=torch.bfloat16) * 1.6)(S, F) * (S ** -0.5)),
            "b1": _w((lambda *s: torch.randn(*s, dtype=torch.bfloat16) * 1.6)(F) * 0.02),
            "w2": _w((lambda *s: torch.randn(*s, dtype=torch.bfloat16) * 0.2)(F, S) * (F ** -0.5)),
            "b2": _w((lambda *s: torch.randn(*s, dtype=torch.bfloat16) * 0.2)(S) * 0.02),
        }
    _B_in = _B_setup()

    return {
        "A_x": _A_in["x"],
        "A_wqkv": _A_in["wqkv"],
        "A_wo": _A_in["wo"],
        "B_x": _B_in["x"],
        "B_w1": _B_in["w1"],
        "B_b1": _B_in["b1"],
        "B_w2": _B_in["w2"],
        "B_b2": _B_in["b2"],
    }

def reference(A_x, A_wqkv, A_wo, B_x, B_w1, B_b1, B_w2, B_b2):
    def _A_ref(x, wqkv, wo):
        B, S, D = x.shape
        H = 32
        Dh = D // H
        qkv = (lambda x, *d: x.permute(*d))((x @ wqkv).reshape(B, S, 3, H, Dh), 2, 0, 3, 1, 4)
        q, k, v = qkv[0], qkv[1], qkv[2]
        scores = torch.einsum("bhqd,bhkd->bhqk", q, k) * (Dh ** -0.5)
        p = _softmax(scores, axis=-1)
        o = torch.einsum("bhqk,bhkd->bhqd", p, v)
        return (lambda x, *d: x.permute(*d))(o, 0, 2, 1, 3).reshape(B, S, D) @ wo

    def _B_ref(x, w1, b1, w2, b2):
        B, S, D = x.shape
        h = _layernorm(x, axis=-1)
        u = torch.einsum("bsd,sf->bdf", h, w1) + b1
        u = _gelu(u)
        y = torch.einsum("bdf,fs->bds", u, w2) + b2
        return (lambda x, *d: x.permute(*d))(y, 0, 2, 1)

    x = A_x
    h = x + _A_ref(_layernorm(x, axis=-1), A_wqkv, A_wo)
    h2 = h + _B_ref(_layernorm(h, axis=-1), B_w1, B_b1, B_w2, B_b2)
    return h2
```

```cpp
#include <hip/hip_runtime.h>
#include <hip/hip_bf16.h>
#include <stdint.h>
#include <type_traits>
#define ATTN_ASM \
    "s_mov_b32 %[sv], m0\n\t" \
    "s_xor_b32 %[so], %[T0], 0\n\t" \
    "s_add_u32 m0, %[ml], 0\n\t" \
    "s_nop 0\n\t" \
    "buffer_load_dwordx4 v167, %[rs], %[so] offen offset:0 sc1 lds\n\t" \
    "buffer_load_dwordx4 v167, %[rs], %[so] offen offset:1024 sc1 lds\n\t" \
    "buffer_load_dwordx4 v167, %[rs], %[so] offen offset:2048 sc1 lds\n\t" \
    "buffer_load_dwordx4 v[96:99], v168, %[rs], 0 offen sc1\n\t" \
    "buffer_load_dwordx2 v[100:101], v169, %[rs], 0 offen sc1\n\t" \
    "buffer_load_dwordx4 v[102:105], v168, %[rs], 0 offen offset:512 sc1\n\t" \
    "buffer_load_dwordx2 v[106:107], v169, %[rs], 0 offen offset:256 sc1\n\t" \
    "v_mov_b32 v0, 0\n\t" \
    "v_mov_b32 v1, 0\n\t" \
    "v_mov_b32 v2, 0\n\t" \
    "v_mov_b32 v3, 0\n\t" \
    "v_mov_b32 v4, 0\n\t" \
    "v_mov_b32 v5, 0\n\t" \
    "v_mov_b32 v6, 0\n\t" \
    "v_mov_b32 v7, 0\n\t" \
    "v_mov_b32 v8, 0\n\t" \
    "v_mov_b32 v9, 0\n\t" \
    "v_mov_b32 v10, 0\n\t" \
    "v_mov_b32 v11, 0\n\t" \
    "v_mov_b32 v12, 0\n\t" \
    "v_mov_b32 v13, 0\n\t" \
    "v_mov_b32 v14, 0\n\t" \
    "v_mov_b32 v15, 0\n\t" \
    "v_mov_b32 v16, 0\n\t" \
    "v_mov_b32 v17, 0\n\t" \
    "v_mov_b32 v18, 0\n\t" \
    "v_mov_b32 v19, 0\n\t" \
    "v_mov_b32 v20, 0\n\t" \
    "v_mov_b32 v21, 0\n\t" \
    "v_mov_b32 v22, 0\n\t" \
    "v_mov_b32 v23, 0\n\t" \
    "v_mov_b32 v24, 0\n\t" \
    "v_mov_b32 v25, 0\n\t" \
    "v_mov_b32 v26, 0\n\t" \
    "v_mov_b32 v27, 0\n\t" \
    "v_mov_b32 v28, 0\n\t" \
    "v_mov_b32 v29, 0\n\t" \
    "v_mov_b32 v30, 0\n\t" \
    "v_mov_b32 v31, 0\n\t" \
    "v_mov_b32 v112, 0\n\t" \
    "v_mov_b32 v113, 0\n\t" \
    "v_mov_b32 v114, 0\n\t" \
    "v_mov_b32 v115, 0\n\t" \
    "v_mov_b32 v120, 0\n\t" \
    "v_mov_b32 v121, 0\n\t" \
    "v_mov_b32 v122, 0\n\t" \
    "v_mov_b32 v123, 0\n\t" \
    "v_mov_b32 v124, 0\n\t" \
    "v_mov_b32 v125, 0\n\t" \
    "v_mov_b32 v126, 0\n\t" \
    "v_mov_b32 v127, 0\n\t" \
    "v_mov_b32 v128, 0\n\t" \
    "v_mov_b32 v129, 0\n\t" \
    "v_mov_b32 v130, 0\n\t" \
    "v_mov_b32 v131, 0\n\t" \
    "v_mov_b32 v160, 0\n\t" \
    "v_mov_b32 v161, 0\n\t" \
    "v_mov_b32 v162, 0\n\t" \
    "v_mov_b32 v163, 0\n\t" \
    "s_waitcnt vmcnt(0)\n\t" \
    "s_xor_b32 %[so], %[T0], 32768\n\t" \
    "s_add_u32 m0, %[ml], 24576\n\t" \
    "s_nop 0\n\t" \
    "buffer_load_dwordx4 v167, %[rs], %[so] offen offset:0 sc1 lds\n\t" \
    "buffer_load_dwordx4 v167, %[rs], %[so] offen offset:1024 sc1 lds\n\t" \
    "buffer_load_dwordx4 v167, %[rs], %[so] offen offset:2048 sc1 lds\n\t" \
    "s_barrier\n\t" \
    "ds_read_b128 v[138:141], v164 offset:0\n\t" \
    "ds_read_b64 v[142:143], v165 offset:0\n\t" \
    "ds_read_b128 v[132:135], v164 offset:512\n\t" \
    "ds_read_b64 v[136:137], v165 offset:256\n\t" \
    "ds_read_b128 v[144:147], v166 offset:0\n\t" \
    "s_waitcnt lgkmcnt(3)\n\t" \
    "v_mfma_f32_32x32x16_bf16 v[32:47], v[138:141], v[96:99], 0\n\t" \
    "v_mfma_f32_32x32x16_bf16 v[64:79], v[138:141], v[102:105], 0\n\t" \
    "v_mfma_f32_32x32x8_bf16 v[32:47], v[142:143], v[100:101], v[32:47]\n\t" \
    "v_mfma_f32_32x32x8_bf16 v[64:79], v[142:143], v[106:107], v[64:79]\n\t" \
    "s_mov_b32 %[J], 0x10000\n\t" \
    "s_nop 7\n\t" \
    "s_nop 3\n\t" \
    "LOOP_%=:\n\t" \
    "s_setprio 3\n\t" \
    "s_waitcnt lgkmcnt(0)\n\t" \
    "v_cvt_pk_bf16_f32 v114, v124, v125\n\t" \
    "v_cvt_pk_bf16_f32 v115, v126, v127\n\t" \
    "v_mfma_f32_32x32x16_bf16 v[48:63], v[132:135], v[96:99], 0\n\t" \
    "ds_read_b128 v[148:151], v166 offset:704\n\t" \
    "v_exp_f32 v32, v32\n\t" \
    "v_exp_f32 v33, v33\n\t" \
    "v_exp_f32 v34, v34\n\t" \
    "v_exp_f32 v35, v35\n\t" \
    "v_mfma_f32_32x32x8_bf16 v[48:63], v[136:137], v[100:101], v[48:63]\n\t" \
    "ds_read_b128 v[152:155], v166 offset:1408\n\t" \
    "v_exp_f32 v36, v36\n\t" \
    "v_cvt_pk_bf16_f32 v108, v32, v33\n\t" \
    "v_exp_f32 v37, v37\n\t" \
    "v_cvt_pk_bf16_f32 v109, v34, v35\n\t" \
    "v_exp_f32 v38, v38\n\t" \
    "v_exp_f32 v39, v39\n\t" \
    "v_mfma_f32_32x32x16_bf16 v[0:15], v[160:163], v[112:115], v[0:15]\n\t" \
    "ds_read_b128 v[156:159], v166 offset:2112\n\t" \
    "v_exp_f32 v40, v40\n\t" \
    "v_cvt_pk_bf16_f32 v110, v36, v37\n\t" \
    "v_exp_f32 v41, v41\n\t" \
    "v_cvt_pk_bf16_f32 v111, v38, v39\n\t" \
    "v_exp_f32 v42, v42\n\t" \
    "v_exp_f32 v43, v43\n\t" \
    "v_mfma_f32_32x32x16_bf16 v[0:15], v[144:147], v[108:111], v[0:15]\n\t" \
    "s_xor_b32 %[so], %[T0], %[J]\n\t" \
    "s_add_u32 m0, %[ml], 0\n\t" \
    "v_exp_f32 v124, v44\n\t" \
    "v_cvt_pk_bf16_f32 v112, v40, v41\n\t" \
    "v_exp_f32 v125, v45\n\t" \
    "v_cvt_pk_bf16_f32 v113, v42, v43\n\t" \
    "v_exp_f32 v126, v46\n\t" \
    "v_exp_f32 v127, v47\n\t" \
    "s_setprio 2\n\t" \
    "s_waitcnt vmcnt(0) lgkmcnt(0)\n\t" \
    "s_barrier\n\t" \
    "s_cmp_lg_u32 %[J], %[JP1]\n\t" \
    "s_cbranch_scc1 NOPF01_%=\n\t" \
    "buffer_load_dword v172, v170, %[rp1], 0 offen\n\t" \
    "NOPF01_%=:\n\t" \
    "s_cmp_lg_u32 %[J], %[JP2]\n\t" \
    "s_cbranch_scc1 NOPF02_%=\n\t" \
    "buffer_load_dword v172, v171, %[rp2], 0 offen\n\t" \
    "NOPF02_%=:\n\t" \
    "v_cvt_pk_bf16_f32 v122, v128, v129\n\t" \
    "v_cvt_pk_bf16_f32 v123, v130, v131\n\t" \
    "v_mfma_f32_32x32x16_bf16 v[80:95], v[132:135], v[102:105], 0\n\t" \
    "ds_read_b128 v[138:141], v164 offset:24576\n\t" \
    "ds_read_b64 v[142:143], v165 offset:24576\n\t" \
    "v_exp_f32 v64, v64\n\t" \
    "v_exp_f32 v65, v65\n\t" \
    "v_exp_f32 v66, v66\n\t" \
    "v_exp_f32 v67, v67\n\t" \
    "v_mfma_f32_32x32x8_bf16 v[80:95], v[136:137], v[106:107], v[80:95]\n\t" \
    "buffer_load_dwordx4 v167, %[rs], %[so] offen offset:0 sc1 lds\n\t" \
    "v_exp_f32 v68, v68\n\t" \
    "v_cvt_pk_bf16_f32 v116, v64, v65\n\t" \
    "v_exp_f32 v69, v69\n\t" \
    "v_cvt_pk_bf16_f32 v117, v66, v67\n\t" \
    "v_exp_f32 v70, v70\n\t" \
    "v_exp_f32 v71, v71\n\t" \
    "v_mfma_f32_32x32x16_bf16 v[16:31], v[160:163], v[120:123], v[16:31]\n\t" \
    "v_exp_f32 v72, v72\n\t" \
    "v_cvt_pk_bf16_f32 v118, v68, v69\n\t" \
    "v_exp_f32 v73, v73\n\t" \
    "v_cvt_pk_bf16_f32 v119, v70, v71\n\t" \
    "v_exp_f32 v74, v74\n\t" \
    "v_exp_f32 v75, v75\n\t" \
    "v_mfma_f32_32x32x16_bf16 v[16:31], v[144:147], v[116:119], v[16:31]\n\t" \
    "buffer_load_dwordx4 v167, %[rs], %[so] offen offset:1024 sc1 lds\n\t" \
    "v_exp_f32 v128, v76\n\t" \
    "v_cvt_pk_bf16_f32 v120, v72, v73\n\t" \
    "v_exp_f32 v129, v77\n\t" \
    "v_cvt_pk_bf16_f32 v121, v74, v75\n\t" \
    "v_exp_f32 v130, v78\n\t" \
    "v_exp_f32 v131, v79\n\t" \
    "s_setprio 1\n\t" \
    "s_waitcnt lgkmcnt(0)\n\t" \
    "v_cvt_pk_bf16_f32 v114, v124, v125\n\t" \
    "v_cvt_pk_bf16_f32 v115, v126, v127\n\t" \
    "v_mfma_f32_32x32x16_bf16 v[32:47], v[138:141], v[96:99], 0\n\t" \
    "ds_read_b128 v[132:135], v164 offset:25088\n\t" \
    "ds_read_b64 v[136:137], v165 offset:24832\n\t" \
    "v_exp_f32 v48, v48\n\t" \
    "v_exp_f32 v49, v49\n\t" \
    "v_exp_f32 v50, v50\n\t" \
    "v_exp_f32 v51, v51\n\t" \
    "v_mfma_f32_32x32x8_bf16 v[32:47], v[142:143], v[100:101], v[32:47]\n\t" \
    "buffer_load_dwordx4 v167, %[rs], %[so] offen offset:2048 sc1 lds\n\t" \
    "v_exp_f32 v52, v52\n\t" \
    "v_cvt_pk_bf16_f32 v108, v48, v49\n\t" \
    "v_exp_f32 v53, v53\n\t" \
    "v_cvt_pk_bf16_f32 v109, v50, v51\n\t" \
    "v_exp_f32 v54, v54\n\t" \
    "v_exp_f32 v55, v55\n\t" \
    "v_mfma_f32_32x32x16_bf16 v[0:15], v[148:151], v[112:115], v[0:15]\n\t" \
    "ds_read_b128 v[144:147], v166 offset:24576\n\t" \
    "v_exp_f32 v56, v56\n\t" \
    "v_cvt_pk_bf16_f32 v110, v52, v53\n\t" \
    "v_exp_f32 v57, v57\n\t" \
    "v_cvt_pk_bf16_f32 v111, v54, v55\n\t" \
    "v_exp_f32 v58, v58\n\t" \
    "v_exp_f32 v59, v59\n\t" \
    "v_mfma_f32_32x32x16_bf16 v[0:15], v[152:155], v[108:111], v[0:15]\n\t" \
    "v_exp_f32 v124, v60\n\t" \
    "v_cvt_pk_bf16_f32 v112, v56, v57\n\t" \
    "v_exp_f32 v125, v61\n\t" \
    "v_cvt_pk_bf16_f32 v113, v58, v59\n\t" \
    "v_exp_f32 v126, v62\n\t" \
    "v_exp_f32 v127, v63\n\t" \
    "s_setprio 0\n\t" \
    "v_cvt_pk_bf16_f32 v122, v128, v129\n\t" \
    "v_cvt_pk_bf16_f32 v123, v130, v131\n\t" \
    "v_mfma_f32_32x32x16_bf16 v[64:79], v[138:141], v[102:105], 0\n\t" \
    "v_exp_f32 v80, v80\n\t" \
    "v_exp_f32 v81, v81\n\t" \
    "v_exp_f32 v82, v82\n\t" \
    "v_exp_f32 v83, v83\n\t" \
    "v_mfma_f32_32x32x8_bf16 v[64:79], v[142:143], v[106:107], v[64:79]\n\t" \
    "v_exp_f32 v84, v84\n\t" \
    "v_cvt_pk_bf16_f32 v116, v80, v81\n\t" \
    "v_exp_f32 v85, v85\n\t" \
    "v_cvt_pk_bf16_f32 v117, v82, v83\n\t" \
    "v_exp_f32 v86, v86\n\t" \
    "v_exp_f32 v87, v87\n\t" \
    "v_mfma_f32_32x32x16_bf16 v[16:31], v[148:151], v[120:123], v[16:31]\n\t" \
    "v_exp_f32 v88, v88\n\t" \
    "v_cvt_pk_bf16_f32 v118, v84, v85\n\t" \
    "v_exp_f32 v89, v89\n\t" \
    "v_cvt_pk_bf16_f32 v119, v86, v87\n\t" \
    "v_exp_f32 v90, v90\n\t" \
    "v_exp_f32 v91, v91\n\t" \
    "v_mfma_f32_32x32x16_bf16 v[16:31], v[152:155], v[116:119], v[16:31]\n\t" \
    "s_add_u32 %[J], %[J], 0x8000\n\t" \
    "v_exp_f32 v128, v92\n\t" \
    "v_cvt_pk_bf16_f32 v120, v88, v89\n\t" \
    "v_exp_f32 v129, v93\n\t" \
    "v_cvt_pk_bf16_f32 v121, v90, v91\n\t" \
    "v_exp_f32 v130, v94\n\t" \
    "v_exp_f32 v131, v95\n\t" \
    "s_setprio 3\n\t" \
    "s_waitcnt lgkmcnt(0)\n\t" \
    "v_cvt_pk_bf16_f32 v114, v124, v125\n\t" \
    "v_cvt_pk_bf16_f32 v115, v126, v127\n\t" \
    "v_mfma_f32_32x32x16_bf16 v[48:63], v[132:135], v[96:99], 0\n\t" \
    "ds_read_b128 v[148:151], v166 offset:25280\n\t" \
    "v_exp_f32 v32, v32\n\t" \
    "v_exp_f32 v33, v33\n\t" \
    "v_exp_f32 v34, v34\n\t" \
    "v_exp_f32 v35, v35\n\t" \
    "v_mfma_f32_32x32x8_bf16 v[48:63], v[136:137], v[100:101], v[48:63]\n\t" \
    "ds_read_b128 v[152:155], v166 offset:25984\n\t" \
    "v_exp_f32 v36, v36\n\t" \
    "v_cvt_pk_bf16_f32 v108, v32, v33\n\t" \
    "v_exp_f32 v37, v37\n\t" \
    "v_cvt_pk_bf16_f32 v109, v34, v35\n\t" \
    "v_exp_f32 v38, v38\n\t" \
    "v_exp_f32 v39, v39\n\t" \
    "v_mfma_f32_32x32x16_bf16 v[0:15], v[156:159], v[112:115], v[0:15]\n\t" \
    "ds_read_b128 v[160:163], v166 offset:26688\n\t" \
    "v_exp_f32 v40, v40\n\t" \
    "v_cvt_pk_bf16_f32 v110, v36, v37\n\t" \
    "v_exp_f32 v41, v41\n\t" \
    "v_cvt_pk_bf16_f32 v111, v38, v39\n\t" \
    "v_exp_f32 v42, v42\n\t" \
    "v_exp_f32 v43, v43\n\t" \
    "v_mfma_f32_32x32x16_bf16 v[0:15], v[144:147], v[108:111], v[0:15]\n\t" \
    "s_xor_b32 %[so], %[T0], %[J]\n\t" \
    "s_add_u32 m0, %[ml], 24576\n\t" \
    "v_exp_f32 v124, v44\n\t" \
    "v_cvt_pk_bf16_f32 v112, v40, v41\n\t" \
    "v_exp_f32 v125, v45\n\t" \
    "v_cvt_pk_bf16_f32 v113, v42, v43\n\t" \
    "v_exp_f32 v126, v46\n\t" \
    "v_exp_f32 v127, v47\n\t" \
    "s_setprio 2\n\t" \
    "s_waitcnt vmcnt(0) lgkmcnt(0)\n\t" \
    "s_barrier\n\t" \
    "s_cmp_lg_u32 %[J], %[JP1]\n\t" \
    "s_cbranch_scc1 NOPF11_%=\n\t" \
    "buffer_load_dword v172, v170, %[rp1], 0 offen\n\t" \
    "NOPF11_%=:\n\t" \
    "s_cmp_lg_u32 %[J], %[JP2]\n\t" \
    "s_cbranch_scc1 NOPF12_%=\n\t" \
    "buffer_load_dword v172, v171, %[rp2], 0 offen\n\t" \
    "NOPF12_%=:\n\t" \
    "v_cvt_pk_bf16_f32 v122, v128, v129\n\t" \
    "v_cvt_pk_bf16_f32 v123, v130, v131\n\t" \
    "v_mfma_f32_32x32x16_bf16 v[80:95], v[132:135], v[102:105], 0\n\t" \
    "ds_read_b128 v[138:141], v164 offset:0\n\t" \
    "ds_read_b64 v[142:143], v165 offset:0\n\t" \
    "v_exp_f32 v64, v64\n\t" \
    "v_exp_f32 v65, v65\n\t" \
    "v_exp_f32 v66, v66\n\t" \
    "v_exp_f32 v67, v67\n\t" \
    "v_mfma_f32_32x32x8_bf16 v[80:95], v[136:137], v[106:107], v[80:95]\n\t" \
    "buffer_load_dwordx4 v167, %[rs], %[so] offen offset:0 sc1 lds\n\t" \
    "v_exp_f32 v68, v68\n\t" \
    "v_cvt_pk_bf16_f32 v116, v64, v65\n\t" \
    "v_exp_f32 v69, v69\n\t" \
    "v_cvt_pk_bf16_f32 v117, v66, v67\n\t" \
    "v_exp_f32 v70, v70\n\t" \
    "v_exp_f32 v71, v71\n\t" \
    "v_mfma_f32_32x32x16_bf16 v[16:31], v[156:159], v[120:123], v[16:31]\n\t" \
    "v_exp_f32 v72, v72\n\t" \
    "v_cvt_pk_bf16_f32 v118, v68, v69\n\t" \
    "v_exp_f32 v73, v73\n\t" \
    "v_cvt_pk_bf16_f32 v119, v70, v71\n\t" \
    "v_exp_f32 v74, v74\n\t" \
    "v_exp_f32 v75, v75\n\t" \
    "v_mfma_f32_32x32x16_bf16 v[16:31], v[144:147], v[116:119], v[16:31]\n\t" \
    "buffer_load_dwordx4 v167, %[rs], %[so] offen offset:1024 sc1 lds\n\t" \
    "v_exp_f32 v128, v76\n\t" \
    "v_cvt_pk_bf16_f32 v120, v72, v73\n\t" \
    "v_exp_f32 v129, v77\n\t" \
    "v_cvt_pk_bf16_f32 v121, v74, v75\n\t" \
    "v_exp_f32 v130, v78\n\t" \
    "v_exp_f32 v131, v79\n\t" \
    "s_setprio 1\n\t" \
    "s_waitcnt lgkmcnt(0)\n\t" \
    "v_cvt_pk_bf16_f32 v114, v124, v125\n\t" \
    "v_cvt_pk_bf16_f32 v115, v126, v127\n\t" \
    "v_mfma_f32_32x32x16_bf16 v[32:47], v[138:141], v[96:99], 0\n\t" \
    "ds_read_b128 v[132:135], v164 offset:512\n\t" \
    "ds_read_b64 v[136:137], v165 offset:256\n\t" \
    "v_exp_f32 v48, v48\n\t" \
    "v_exp_f32 v49, v49\n\t" \
    "v_exp_f32 v50, v50\n\t" \
    "v_exp_f32 v51, v51\n\t" \
    "v_mfma_f32_32x32x8_bf16 v[32:47], v[142:143], v[100:101], v[32:47]\n\t" \
    "buffer_load_dwordx4 v167, %[rs], %[so] offen offset:2048 sc1 lds\n\t" \
    "v_exp_f32 v52, v52\n\t" \
    "v_cvt_pk_bf16_f32 v108, v48, v49\n\t" \
    "v_exp_f32 v53, v53\n\t" \
    "v_cvt_pk_bf16_f32 v109, v50, v51\n\t" \
    "v_exp_f32 v54, v54\n\t" \
    "v_exp_f32 v55, v55\n\t" \
    "v_mfma_f32_32x32x16_bf16 v[0:15], v[148:151], v[112:115], v[0:15]\n\t" \
    "ds_read_b128 v[144:147], v166 offset:0\n\t" \
    "v_exp_f32 v56, v56\n\t" \
    "v_cvt_pk_bf16_f32 v110, v52, v53\n\t" \
    "v_exp_f32 v57, v57\n\t" \
    "v_cvt_pk_bf16_f32 v111, v54, v55\n\t" \
    "v_exp_f32 v58, v58\n\t" \
    "v_exp_f32 v59, v59\n\t" \
    "v_mfma_f32_32x32x16_bf16 v[0:15], v[152:155], v[108:111], v[0:15]\n\t" \
    "v_exp_f32 v124, v60\n\t" \
    "v_cvt_pk_bf16_f32 v112, v56, v57\n\t" \
    "v_exp_f32 v125, v61\n\t" \
    "v_cvt_pk_bf16_f32 v113, v58, v59\n\t" \
    "v_exp_f32 v126, v62\n\t" \
    "v_exp_f32 v127, v63\n\t" \
    "s_setprio 0\n\t" \
    "v_cvt_pk_bf16_f32 v122, v128, v129\n\t" \
    "v_cvt_pk_bf16_f32 v123, v130, v131\n\t" \
    "v_mfma_f32_32x32x16_bf16 v[64:79], v[138:141], v[102:105], 0\n\t" \
    "v_exp_f32 v80, v80\n\t" \
    "v_exp_f32 v81, v81\n\t" \
    "v_exp_f32 v82, v82\n\t" \
    "v_exp_f32 v83, v83\n\t" \
    "v_mfma_f32_32x32x8_bf16 v[64:79], v[142:143], v[106:107], v[64:79]\n\t" \
    "v_exp_f32 v84, v84\n\t" \
    "v_cvt_pk_bf16_f32 v116, v80, v81\n\t" \
    "v_exp_f32 v85, v85\n\t" \
    "v_cvt_pk_bf16_f32 v117, v82, v83\n\t" \
    "v_exp_f32 v86, v86\n\t" \
    "v_exp_f32 v87, v87\n\t" \
    "v_mfma_f32_32x32x16_bf16 v[16:31], v[148:151], v[120:123], v[16:31]\n\t" \
    "v_exp_f32 v88, v88\n\t" \
    "v_cvt_pk_bf16_f32 v118, v84, v85\n\t" \
    "v_exp_f32 v89, v89\n\t" \
    "v_cvt_pk_bf16_f32 v119, v86, v87\n\t" \
    "v_exp_f32 v90, v90\n\t" \
    "v_exp_f32 v91, v91\n\t" \
    "v_mfma_f32_32x32x16_bf16 v[16:31], v[152:155], v[116:119], v[16:31]\n\t" \
    "s_add_u32 %[J], %[J], 0x8000\n\t" \
    "v_exp_f32 v128, v92\n\t" \
    "v_cvt_pk_bf16_f32 v120, v88, v89\n\t" \
    "v_exp_f32 v129, v93\n\t" \
    "v_cvt_pk_bf16_f32 v121, v90, v91\n\t" \
    "v_exp_f32 v130, v94\n\t" \
    "v_exp_f32 v131, v95\n\t" \
    "s_cmp_lt_u32 %[J], 524288\n\t" \
    "s_cbranch_scc1 LOOP_%=\n\t" \
    "s_setprio 3\n\t" \
    "s_waitcnt lgkmcnt(0)\n\t" \
    "v_cvt_pk_bf16_f32 v114, v124, v125\n\t" \
    "v_cvt_pk_bf16_f32 v115, v126, v127\n\t" \
    "v_mfma_f32_32x32x16_bf16 v[48:63], v[132:135], v[96:99], 0\n\t" \
    "ds_read_b128 v[148:151], v166 offset:704\n\t" \
    "v_exp_f32 v32, v32\n\t" \
    "v_exp_f32 v33, v33\n\t" \
    "v_exp_f32 v34, v34\n\t" \
    "v_exp_f32 v35, v35\n\t" \
    "v_mfma_f32_32x32x8_bf16 v[48:63], v[136:137], v[100:101], v[48:63]\n\t" \
    "ds_read_b128 v[152:155], v166 offset:1408\n\t" \
    "v_exp_f32 v36, v36\n\t" \
    "v_cvt_pk_bf16_f32 v108, v32, v33\n\t" \
    "v_exp_f32 v37, v37\n\t" \
    "v_cvt_pk_bf16_f32 v109, v34, v35\n\t" \
    "v_exp_f32 v38, v38\n\t" \
    "v_exp_f32 v39, v39\n\t" \
    "v_mfma_f32_32x32x16_bf16 v[0:15], v[160:163], v[112:115], v[0:15]\n\t" \
    "ds_read_b128 v[156:159], v166 offset:2112\n\t" \
    "v_exp_f32 v40, v40\n\t" \
    "v_cvt_pk_bf16_f32 v110, v36, v37\n\t" \
    "v_exp_f32 v41, v41\n\t" \
    "v_cvt_pk_bf16_f32 v111, v38, v39\n\t" \
    "v_exp_f32 v42, v42\n\t" \
    "v_exp_f32 v43, v43\n\t" \
    "v_mfma_f32_32x32x16_bf16 v[0:15], v[144:147], v[108:111], v[0:15]\n\t" \
    "v_exp_f32 v124, v44\n\t" \
    "v_cvt_pk_bf16_f32 v112, v40, v41\n\t" \
    "v_exp_f32 v125, v45\n\t" \
    "v_cvt_pk_bf16_f32 v113, v42, v43\n\t" \
    "v_exp_f32 v126, v46\n\t" \
    "v_exp_f32 v127, v47\n\t" \
    "s_setprio 2\n\t" \
    "s_waitcnt vmcnt(0) lgkmcnt(0)\n\t" \
    "s_barrier\n\t" \
    "v_cvt_pk_bf16_f32 v122, v128, v129\n\t" \
    "v_cvt_pk_bf16_f32 v123, v130, v131\n\t" \
    "v_mfma_f32_32x32x16_bf16 v[80:95], v[132:135], v[102:105], 0\n\t" \
    "ds_read_b128 v[138:141], v164 offset:24576\n\t" \
    "ds_read_b64 v[142:143], v165 offset:24576\n\t" \
    "v_exp_f32 v64, v64\n\t" \
    "v_exp_f32 v65, v65\n\t" \
    "v_exp_f32 v66, v66\n\t" \
    "v_exp_f32 v67, v67\n\t" \
    "v_mfma_f32_32x32x8_bf16 v[80:95], v[136:137], v[106:107], v[80:95]\n\t" \
    "v_exp_f32 v68, v68\n\t" \
    "v_cvt_pk_bf16_f32 v116, v64, v65\n\t" \
    "v_exp_f32 v69, v69\n\t" \
    "v_cvt_pk_bf16_f32 v117, v66, v67\n\t" \
    "v_exp_f32 v70, v70\n\t" \
    "v_exp_f32 v71, v71\n\t" \
    "v_mfma_f32_32x32x16_bf16 v[16:31], v[160:163], v[120:123], v[16:31]\n\t" \
    "v_exp_f32 v72, v72\n\t" \
    "v_cvt_pk_bf16_f32 v118, v68, v69\n\t" \
    "v_exp_f32 v73, v73\n\t" \
    "v_cvt_pk_bf16_f32 v119, v70, v71\n\t" \
    "v_exp_f32 v74, v74\n\t" \
    "v_exp_f32 v75, v75\n\t" \
    "v_mfma_f32_32x32x16_bf16 v[16:31], v[144:147], v[116:119], v[16:31]\n\t" \
    "v_exp_f32 v128, v76\n\t" \
    "v_cvt_pk_bf16_f32 v120, v72, v73\n\t" \
    "v_exp_f32 v129, v77\n\t" \
    "v_cvt_pk_bf16_f32 v121, v74, v75\n\t" \
    "v_exp_f32 v130, v78\n\t" \
    "v_exp_f32 v131, v79\n\t" \
    "s_setprio 1\n\t" \
    "s_waitcnt lgkmcnt(0)\n\t" \
    "v_cvt_pk_bf16_f32 v114, v124, v125\n\t" \
    "v_cvt_pk_bf16_f32 v115, v126, v127\n\t" \
    "v_mfma_f32_32x32x16_bf16 v[32:47], v[138:141], v[96:99], 0\n\t" \
    "ds_read_b128 v[132:135], v164 offset:25088\n\t" \
    "ds_read_b64 v[136:137], v165 offset:24832\n\t" \
    "v_exp_f32 v48, v48\n\t" \
    "v_exp_f32 v49, v49\n\t" \
    "v_exp_f32 v50, v50\n\t" \
    "v_exp_f32 v51, v51\n\t" \
    "v_mfma_f32_32x32x8_bf16 v[32:47], v[142:143], v[100:101], v[32:47]\n\t" \
    "v_exp_f32 v52, v52\n\t" \
    "v_cvt_pk_bf16_f32 v108, v48, v49\n\t" \
    "v_exp_f32 v53, v53\n\t" \
    "v_cvt_pk_bf16_f32 v109, v50, v51\n\t" \
    "v_exp_f32 v54, v54\n\t" \
    "v_exp_f32 v55, v55\n\t" \
    "v_mfma_f32_32x32x16_bf16 v[0:15], v[148:151], v[112:115], v[0:15]\n\t" \
    "ds_read_b128 v[144:147], v166 offset:24576\n\t" \
    "v_exp_f32 v56, v56\n\t" \
    "v_cvt_pk_bf16_f32 v110, v52, v53\n\t" \
    "v_exp_f32 v57, v57\n\t" \
    "v_cvt_pk_bf16_f32 v111, v54, v55\n\t" \
    "v_exp_f32 v58, v58\n\t" \
    "v_exp_f32 v59, v59\n\t" \
    "v_mfma_f32_32x32x16_bf16 v[0:15], v[152:155], v[108:111], v[0:15]\n\t" \
    "v_exp_f32 v124, v60\n\t" \
    "v_cvt_pk_bf16_f32 v112, v56, v57\n\t" \
    "v_exp_f32 v125, v61\n\t" \
    "v_cvt_pk_bf16_f32 v113, v58, v59\n\t" \
    "v_exp_f32 v126, v62\n\t" \
    "v_exp_f32 v127, v63\n\t" \
    "s_setprio 0\n\t" \
    "v_cvt_pk_bf16_f32 v122, v128, v129\n\t" \
    "v_cvt_pk_bf16_f32 v123, v130, v131\n\t" \
    "v_mfma_f32_32x32x16_bf16 v[64:79], v[138:141], v[102:105], 0\n\t" \
    "v_exp_f32 v80, v80\n\t" \
    "v_exp_f32 v81, v81\n\t" \
    "v_exp_f32 v82, v82\n\t" \
    "v_exp_f32 v83, v83\n\t" \
    "v_mfma_f32_32x32x8_bf16 v[64:79], v[142:143], v[106:107], v[64:79]\n\t" \
    "v_exp_f32 v84, v84\n\t" \
    "v_cvt_pk_bf16_f32 v116, v80, v81\n\t" \
    "v_exp_f32 v85, v85\n\t" \
    "v_cvt_pk_bf16_f32 v117, v82, v83\n\t" \
    "v_exp_f32 v86, v86\n\t" \
    "v_exp_f32 v87, v87\n\t" \
    "v_mfma_f32_32x32x16_bf16 v[16:31], v[148:151], v[120:123], v[16:31]\n\t" \
    "v_exp_f32 v88, v88\n\t" \
    "v_cvt_pk_bf16_f32 v118, v84, v85\n\t" \
    "v_exp_f32 v89, v89\n\t" \
    "v_cvt_pk_bf16_f32 v119, v86, v87\n\t" \
    "v_exp_f32 v90, v90\n\t" \
    "v_exp_f32 v91, v91\n\t" \
    "v_mfma_f32_32x32x16_bf16 v[16:31], v[152:155], v[116:119], v[16:31]\n\t" \
    "s_add_u32 %[J], %[J], 0x8000\n\t" \
    "v_exp_f32 v128, v92\n\t" \
    "v_cvt_pk_bf16_f32 v120, v88, v89\n\t" \
    "v_exp_f32 v129, v93\n\t" \
    "v_cvt_pk_bf16_f32 v121, v90, v91\n\t" \
    "v_exp_f32 v130, v94\n\t" \
    "v_exp_f32 v131, v95\n\t" \
    "s_setprio 3\n\t" \
    "s_waitcnt lgkmcnt(0)\n\t" \
    "v_cvt_pk_bf16_f32 v114, v124, v125\n\t" \
    "v_cvt_pk_bf16_f32 v115, v126, v127\n\t" \
    "v_mfma_f32_32x32x16_bf16 v[48:63], v[132:135], v[96:99], 0\n\t" \
    "ds_read_b128 v[148:151], v166 offset:25280\n\t" \
    "v_exp_f32 v32, v32\n\t" \
    "v_exp_f32 v33, v33\n\t" \
    "v_exp_f32 v34, v34\n\t" \
    "v_exp_f32 v35, v35\n\t" \
    "v_mfma_f32_32x32x8_bf16 v[48:63], v[136:137], v[100:101], v[48:63]\n\t" \
    "ds_read_b128 v[152:155], v166 offset:25984\n\t" \
    "v_exp_f32 v36, v36\n\t" \
    "v_cvt_pk_bf16_f32 v108, v32, v33\n\t" \
    "v_exp_f32 v37, v37\n\t" \
    "v_cvt_pk_bf16_f32 v109, v34, v35\n\t" \
    "v_exp_f32 v38, v38\n\t" \
    "v_exp_f32 v39, v39\n\t" \
    "v_mfma_f32_32x32x16_bf16 v[0:15], v[156:159], v[112:115], v[0:15]\n\t" \
    "ds_read_b128 v[160:163], v166 offset:26688\n\t" \
    "v_exp_f32 v40, v40\n\t" \
    "v_cvt_pk_bf16_f32 v110, v36, v37\n\t" \
    "v_exp_f32 v41, v41\n\t" \
    "v_cvt_pk_bf16_f32 v111, v38, v39\n\t" \
    "v_exp_f32 v42, v42\n\t" \
    "v_exp_f32 v43, v43\n\t" \
    "v_mfma_f32_32x32x16_bf16 v[0:15], v[144:147], v[108:111], v[0:15]\n\t" \
    "v_exp_f32 v124, v44\n\t" \
    "v_cvt_pk_bf16_f32 v112, v40, v41\n\t" \
    "v_exp_f32 v125, v45\n\t" \
    "v_cvt_pk_bf16_f32 v113, v42, v43\n\t" \
    "v_exp_f32 v126, v46\n\t" \
    "v_exp_f32 v127, v47\n\t" \
    "s_setprio 2\n\t" \
    "s_waitcnt vmcnt(0) lgkmcnt(0)\n\t" \
    "s_barrier\n\t" \
    "v_cvt_pk_bf16_f32 v122, v128, v129\n\t" \
    "v_cvt_pk_bf16_f32 v123, v130, v131\n\t" \
    "v_mfma_f32_32x32x16_bf16 v[80:95], v[132:135], v[102:105], 0\n\t" \
    "v_exp_f32 v64, v64\n\t" \
    "v_exp_f32 v65, v65\n\t" \
    "v_exp_f32 v66, v66\n\t" \
    "v_exp_f32 v67, v67\n\t" \
    "v_mfma_f32_32x32x8_bf16 v[80:95], v[136:137], v[106:107], v[80:95]\n\t" \
    "v_exp_f32 v68, v68\n\t" \
    "v_cvt_pk_bf16_f32 v116, v64, v65\n\t" \
    "v_exp_f32 v69, v69\n\t" \
    "v_cvt_pk_bf16_f32 v117, v66, v67\n\t" \
    "v_exp_f32 v70, v70\n\t" \
    "v_exp_f32 v71, v71\n\t" \
    "v_mfma_f32_32x32x16_bf16 v[16:31], v[156:159], v[120:123], v[16:31]\n\t" \
    "v_exp_f32 v72, v72\n\t" \
    "v_cvt_pk_bf16_f32 v118, v68, v69\n\t" \
    "v_exp_f32 v73, v73\n\t" \
    "v_cvt_pk_bf16_f32 v119, v70, v71\n\t" \
    "v_exp_f32 v74, v74\n\t" \
    "v_exp_f32 v75, v75\n\t" \
    "v_mfma_f32_32x32x16_bf16 v[16:31], v[144:147], v[116:119], v[16:31]\n\t" \
    "v_exp_f32 v128, v76\n\t" \
    "v_cvt_pk_bf16_f32 v120, v72, v73\n\t" \
    "v_exp_f32 v129, v77\n\t" \
    "v_cvt_pk_bf16_f32 v121, v74, v75\n\t" \
    "v_exp_f32 v130, v78\n\t" \
    "v_exp_f32 v131, v79\n\t" \
    "s_setprio 1\n\t" \
    "s_waitcnt lgkmcnt(0)\n\t" \
    "v_cvt_pk_bf16_f32 v114, v124, v125\n\t" \
    "v_cvt_pk_bf16_f32 v115, v126, v127\n\t" \
    "v_exp_f32 v48, v48\n\t" \
    "v_exp_f32 v49, v49\n\t" \
    "v_exp_f32 v50, v50\n\t" \
    "v_exp_f32 v51, v51\n\t" \
    "v_exp_f32 v52, v52\n\t" \
    "v_cvt_pk_bf16_f32 v108, v48, v49\n\t" \
    "v_exp_f32 v53, v53\n\t" \
    "v_cvt_pk_bf16_f32 v109, v50, v51\n\t" \
    "v_exp_f32 v54, v54\n\t" \
    "v_exp_f32 v55, v55\n\t" \
    "v_mfma_f32_32x32x16_bf16 v[0:15], v[148:151], v[112:115], v[0:15]\n\t" \
    "v_exp_f32 v56, v56\n\t" \
    "v_cvt_pk_bf16_f32 v110, v52, v53\n\t" \
    "v_exp_f32 v57, v57\n\t" \
    "v_cvt_pk_bf16_f32 v111, v54, v55\n\t" \
    "v_exp_f32 v58, v58\n\t" \
    "v_exp_f32 v59, v59\n\t" \
    "v_mfma_f32_32x32x16_bf16 v[0:15], v[152:155], v[108:111], v[0:15]\n\t" \
    "v_exp_f32 v124, v60\n\t" \
    "v_cvt_pk_bf16_f32 v112, v56, v57\n\t" \
    "v_exp_f32 v125, v61\n\t" \
    "v_cvt_pk_bf16_f32 v113, v58, v59\n\t" \
    "v_exp_f32 v126, v62\n\t" \
    "v_exp_f32 v127, v63\n\t" \
    "s_setprio 0\n\t" \
    "v_cvt_pk_bf16_f32 v122, v128, v129\n\t" \
    "v_cvt_pk_bf16_f32 v123, v130, v131\n\t" \
    "v_exp_f32 v80, v80\n\t" \
    "v_exp_f32 v81, v81\n\t" \
    "v_exp_f32 v82, v82\n\t" \
    "v_exp_f32 v83, v83\n\t" \
    "v_exp_f32 v84, v84\n\t" \
    "v_cvt_pk_bf16_f32 v116, v80, v81\n\t" \
    "v_exp_f32 v85, v85\n\t" \
    "v_cvt_pk_bf16_f32 v117, v82, v83\n\t" \
    "v_exp_f32 v86, v86\n\t" \
    "v_exp_f32 v87, v87\n\t" \
    "v_mfma_f32_32x32x16_bf16 v[16:31], v[148:151], v[120:123], v[16:31]\n\t" \
    "v_exp_f32 v88, v88\n\t" \
    "v_cvt_pk_bf16_f32 v118, v84, v85\n\t" \
    "v_exp_f32 v89, v89\n\t" \
    "v_cvt_pk_bf16_f32 v119, v86, v87\n\t" \
    "v_exp_f32 v90, v90\n\t" \
    "v_exp_f32 v91, v91\n\t" \
    "v_mfma_f32_32x32x16_bf16 v[16:31], v[152:155], v[116:119], v[16:31]\n\t" \
    "s_add_u32 %[J], %[J], 0x8000\n\t" \
    "v_exp_f32 v128, v92\n\t" \
    "v_cvt_pk_bf16_f32 v120, v88, v89\n\t" \
    "v_exp_f32 v129, v93\n\t" \
    "v_cvt_pk_bf16_f32 v121, v90, v91\n\t" \
    "v_exp_f32 v130, v94\n\t" \
    "v_exp_f32 v131, v95\n\t" \
    "v_cvt_pk_bf16_f32 v114, v124, v125\n\t" \
    "v_cvt_pk_bf16_f32 v115, v126, v127\n\t" \
    "v_cvt_pk_bf16_f32 v122, v128, v129\n\t" \
    "v_cvt_pk_bf16_f32 v123, v130, v131\n\t" \
    "s_nop 1\n\t" \
    "v_mfma_f32_32x32x16_bf16 v[0:15], v[160:163], v[112:115], v[0:15]\n\t" \
    "v_mfma_f32_32x32x16_bf16 v[16:31], v[160:163], v[120:123], v[16:31]\n\t" \
    "s_setprio 0\n\t" \
    "s_nop 15\n\t" \
    "s_mov_b32 m0, %[sv]\n\t"
#define ATTN_CLOB "v32","v33","v34","v35","v36","v37","v38","v39","v40","v41","v42","v43","v44","v45","v46","v47","v48","v49","v50","v51","v52","v53","v54","v55","v56","v57","v58","v59","v60","v61","v62","v63","v64","v65","v66","v67","v68","v69","v70","v71","v72","v73","v74","v75","v76","v77","v78","v79","v80","v81","v82","v83","v84","v85","v86","v87","v88","v89","v90","v91","v92","v93","v94","v95","v96","v97","v98","v99","v100","v101","v102","v103","v104","v105","v106","v107","v108","v109","v110","v111","v112","v113","v114","v115","v116","v117","v118","v119","v120","v121","v122","v123","v124","v125","v126","v127","v128","v129","v130","v131","v132","v133","v134","v135","v136","v137","v138","v139","v140","v141","v142","v143","v144","v145","v146","v147","v148","v149","v150","v151","v152","v153","v154","v155","v156","v157","v158","v159","v160","v161","v162","v163","v172"
#define QKV_PRO0_ASM \
    "s_mov_b32 %[sv], m0\n\t" \
    "s_add_u32 m0, %[mx], 0\n\t" \
    "s_mov_b32 %[sx], 0\n\t" \
    "buffer_load_dwordx4 v184, %[rx], %[sx] offen lds\n\t" \
    "buffer_load_dwordx4 v185, %[rx], %[sx] offen offset:1024 lds\n\t" \
    "s_add_u32 m0, %[mx], 16384\n\t" \
    "s_mov_b32 %[sw], 0\n\t" \
    "buffer_load_dwordx4 v186, %[rw], %[sw] offen lds\n\t" \
    "buffer_load_dwordx4 v187, %[rw], %[sw] offen offset:1024 lds\n\t" \
    "s_mov_b32 m0, %[sv]\n\t"
#define QKV_PRO1_ASM \
    "s_mov_b32 %[sv], m0\n\t" \
    "s_add_u32 m0, %[mx], 32768\n\t" \
    "s_mov_b32 %[sx], 128\n\t" \
    "buffer_load_dwordx4 v184, %[rx], %[sx] offen lds\n\t" \
    "buffer_load_dwordx4 v185, %[rx], %[sx] offen offset:1024 lds\n\t" \
    "s_add_u32 m0, %[mx], 49152\n\t" \
    "s_mov_b32 %[sw], 245760\n\t" \
    "buffer_load_dwordx4 v186, %[rw], %[sw] offen lds\n\t" \
    "buffer_load_dwordx4 v187, %[rw], %[sw] offen offset:1024 lds\n\t" \
    "s_mov_b32 m0, %[sv]\n\t"
#define QKV_PRO2_ASM \
    "s_mov_b32 %[sv], m0\n\t" \
    "s_add_u32 m0, %[mx], 65536\n\t" \
    "s_mov_b32 %[sx], 256\n\t" \
    "buffer_load_dwordx4 v184, %[rx], %[sx] offen lds\n\t" \
    "buffer_load_dwordx4 v185, %[rx], %[sx] offen offset:1024 lds\n\t" \
    "s_add_u32 m0, %[mx], 81920\n\t" \
    "s_mov_b32 %[sw], 491520\n\t" \
    "buffer_load_dwordx4 v186, %[rw], %[sw] offen lds\n\t" \
    "buffer_load_dwordx4 v187, %[rw], %[sw] offen offset:1024 lds\n\t" \
    "s_mov_b32 m0, %[sv]\n\t"
#define QKV_ASM \
    "s_mov_b32 %[sv], m0\n\t" \
    "v_mov_b32 v0, 0\n\t" \
    "v_mov_b32 v1, 0\n\t" \
    "v_mov_b32 v2, 0\n\t" \
    "v_mov_b32 v3, 0\n\t" \
    "v_mov_b32 v4, 0\n\t" \
    "v_mov_b32 v5, 0\n\t" \
    "v_mov_b32 v6, 0\n\t" \
    "v_mov_b32 v7, 0\n\t" \
    "v_mov_b32 v8, 0\n\t" \
    "v_mov_b32 v9, 0\n\t" \
    "v_mov_b32 v10, 0\n\t" \
    "v_mov_b32 v11, 0\n\t" \
    "v_mov_b32 v12, 0\n\t" \
    "v_mov_b32 v13, 0\n\t" \
    "v_mov_b32 v14, 0\n\t" \
    "v_mov_b32 v15, 0\n\t" \
    "v_mov_b32 v16, 0\n\t" \
    "v_mov_b32 v17, 0\n\t" \
    "v_mov_b32 v18, 0\n\t" \
    "v_mov_b32 v19, 0\n\t" \
    "v_mov_b32 v20, 0\n\t" \
    "v_mov_b32 v21, 0\n\t" \
    "v_mov_b32 v22, 0\n\t" \
    "v_mov_b32 v23, 0\n\t" \
    "v_mov_b32 v24, 0\n\t" \
    "v_mov_b32 v25, 0\n\t" \
    "v_mov_b32 v26, 0\n\t" \
    "v_mov_b32 v27, 0\n\t" \
    "v_mov_b32 v28, 0\n\t" \
    "v_mov_b32 v29, 0\n\t" \
    "v_mov_b32 v30, 0\n\t" \
    "v_mov_b32 v31, 0\n\t" \
    "v_mov_b32 v32, 0\n\t" \
    "v_mov_b32 v33, 0\n\t" \
    "v_mov_b32 v34, 0\n\t" \
    "v_mov_b32 v35, 0\n\t" \
    "v_mov_b32 v36, 0\n\t" \
    "v_mov_b32 v37, 0\n\t" \
    "v_mov_b32 v38, 0\n\t" \
    "v_mov_b32 v39, 0\n\t" \
    "v_mov_b32 v40, 0\n\t" \
    "v_mov_b32 v41, 0\n\t" \
    "v_mov_b32 v42, 0\n\t" \
    "v_mov_b32 v43, 0\n\t" \
    "v_mov_b32 v44, 0\n\t" \
    "v_mov_b32 v45, 0\n\t" \
    "v_mov_b32 v46, 0\n\t" \
    "v_mov_b32 v47, 0\n\t" \
    "v_mov_b32 v48, 0\n\t" \
    "v_mov_b32 v49, 0\n\t" \
    "v_mov_b32 v50, 0\n\t" \
    "v_mov_b32 v51, 0\n\t" \
    "v_mov_b32 v58, 0\n\t" \
    "v_mov_b32 v59, 0\n\t" \
    "v_mov_b32 v60, 0\n\t" \
    "v_mov_b32 v61, 0\n\t" \
    "v_mov_b32 v190, 0\n\t" \
    "v_mov_b32 v191, 0\n\t" \
    "v_mov_b32 v192, 0\n\t" \
    "v_mov_b32 v193, 0\n\t" \
    "v_mov_b32 v56, 0xffff0000\n\t" \
    "v_mov_b32 v62, 0x3f803f80\n\t" \
    "v_mov_b32 v63, 0x3f803f80\n\t" \
    "s_waitcnt vmcnt(8) lgkmcnt(0)\n\t" \
    "s_barrier\n\t" \
    "s_add_u32 m0, %[mx], 98304\n\t" \
    "s_mov_b32 %[sx], 384\n\t" \
    "buffer_load_dwordx4 v184, %[rx], %[sx] offen lds\n\t" \
    "buffer_load_dwordx4 v185, %[rx], %[sx] offen offset:1024 lds\n\t" \
    "s_add_u32 m0, %[mx], 114688\n\t" \
    "s_mov_b32 %[sw], 737280\n\t" \
    "buffer_load_dwordx4 v186, %[rw], %[sw] offen lds\n\t" \
    "buffer_load_dwordx4 v187, %[rw], %[sw] offen offset:1024 lds\n\t" \
    "ds_read_b128 v[64:67], v160 offset:0\n\t" \
    "ds_read_b64_tr_b16 v[96:97], v168 offset:0\n\t" \
    "ds_read_b64_tr_b16 v[98:99], v168 offset:1024\n\t" \
    "ds_read_b64_tr_b16 v[112:113], v172 offset:0\n\t" \
    "ds_read_b64_tr_b16 v[114:115], v172 offset:1024\n\t" \
    "ds_read_b128 v[68:71], v161 offset:0\n\t" \
    "ds_read_b64_tr_b16 v[100:101], v169 offset:0\n\t" \
    "ds_read_b64_tr_b16 v[102:103], v169 offset:1024\n\t" \
    "ds_read_b64_tr_b16 v[116:117], v173 offset:0\n\t" \
    "ds_read_b64_tr_b16 v[118:119], v173 offset:1024\n\t" \
    "ds_read_b128 v[72:75], v162 offset:0\n\t" \
    "ds_read_b64_tr_b16 v[104:105], v170 offset:0\n\t" \
    "ds_read_b64_tr_b16 v[106:107], v170 offset:1024\n\t" \
    "ds_read_b64_tr_b16 v[120:121], v174 offset:0\n\t" \
    "ds_read_b64_tr_b16 v[122:123], v174 offset:1024\n\t" \
    "ds_read_b128 v[76:79], v163 offset:0\n\t" \
    "ds_read_b64_tr_b16 v[108:109], v171 offset:0\n\t" \
    "ds_read_b64_tr_b16 v[110:111], v171 offset:1024\n\t" \
    "ds_read_b64_tr_b16 v[124:125], v175 offset:0\n\t" \
    "ds_read_b64_tr_b16 v[126:127], v175 offset:1024\n\t" \
    "s_waitcnt vmcnt(8) lgkmcnt(0)\n\t" \
    "s_barrier\n\t" \
    "v_mfma_f32_32x32x16_bf16 v[0:15], v[96:99], v[64:67], v[0:15]\n\t" \
    "s_add_u32 m0, %[mx], 0\n\t" \
    "s_mov_b32 %[sx], 512\n\t" \
    "buffer_load_dwordx4 v184, %[rx], %[sx] offen lds\n\t" \
    "buffer_load_dwordx4 v185, %[rx], %[sx] offen offset:1024 lds\n\t" \
    "v_lshlrev_b32 v52, 16, v96\n\t" \
    "v_and_b32 v53, v56, v96\n\t" \
    "v_lshlrev_b32 v54, 16, v112\n\t" \
    "v_and_b32 v55, v56, v112\n\t" \
    "v_mfma_f32_32x32x16_bf16 v[16:31], v[112:115], v[64:67], v[16:31]\n\t" \
    "s_add_u32 m0, %[mx], 16384\n\t" \
    "s_mov_b32 %[sw], 983040\n\t" \
    "buffer_load_dwordx4 v186, %[rw], %[sw] offen lds\n\t" \
    "buffer_load_dwordx4 v187, %[rw], %[sw] offen offset:1024 lds\n\t" \
    "v_add_f32 v48, v48, v52\n\t" \
    "v_add_f32 v49, v49, v53\n\t" \
    "v_add_f32 v50, v50, v54\n\t" \
    "v_add_f32 v51, v51, v55\n\t" \
    "v_mfma_f32_32x32x16_bf16 v[0:15], v[100:103], v[68:71], v[0:15]\n\t" \
    "ds_read_b128 v[80:83], v160 offset:32768\n\t" \
    "ds_read_b64_tr_b16 v[128:129], v168 offset:32768\n\t" \
    "ds_read_b64_tr_b16 v[130:131], v168 offset:33792\n\t" \
    "ds_read_b64_tr_b16 v[144:145], v172 offset:32768\n\t" \
    "v_lshlrev_b32 v52, 16, v97\n\t" \
    "v_and_b32 v53, v56, v97\n\t" \
    "v_lshlrev_b32 v54, 16, v113\n\t" \
    "v_and_b32 v55, v56, v113\n\t" \
    "v_mfma_f32_32x32x16_bf16 v[16:31], v[116:119], v[68:71], v[16:31]\n\t" \
    "ds_read_b64_tr_b16 v[146:147], v172 offset:33792\n\t" \
    "ds_read_b128 v[84:87], v161 offset:32768\n\t" \
    "ds_read_b64_tr_b16 v[132:133], v169 offset:32768\n\t" \
    "ds_read_b64_tr_b16 v[134:135], v169 offset:33792\n\t" \
    "v_add_f32 v48, v48, v52\n\t" \
    "v_add_f32 v49, v49, v53\n\t" \
    "v_add_f32 v50, v50, v54\n\t" \
    "v_add_f32 v51, v51, v55\n\t" \
    "v_mfma_f32_32x32x16_bf16 v[0:15], v[104:107], v[72:75], v[0:15]\n\t" \
    "ds_read_b64_tr_b16 v[148:149], v173 offset:32768\n\t" \
    "ds_read_b64_tr_b16 v[150:151], v173 offset:33792\n\t" \
    "ds_read_b128 v[88:91], v162 offset:32768\n\t" \
    "ds_read_b64_tr_b16 v[136:137], v170 offset:32768\n\t" \
    "v_lshlrev_b32 v52, 16, v98\n\t" \
    "v_and_b32 v53, v56, v98\n\t" \
    "v_lshlrev_b32 v54, 16, v114\n\t" \
    "v_and_b32 v55, v56, v114\n\t" \
    "v_mfma_f32_32x32x16_bf16 v[16:31], v[120:123], v[72:75], v[16:31]\n\t" \
    "ds_read_b64_tr_b16 v[138:139], v170 offset:33792\n\t" \
    "ds_read_b64_tr_b16 v[152:153], v174 offset:32768\n\t" \
    "ds_read_b64_tr_b16 v[154:155], v174 offset:33792\n\t" \
    "ds_read_b128 v[92:95], v163 offset:32768\n\t" \
    "v_add_f32 v48, v48, v52\n\t" \
    "v_add_f32 v49, v49, v53\n\t" \
    "v_add_f32 v50, v50, v54\n\t" \
    "v_add_f32 v51, v51, v55\n\t" \
    "v_mfma_f32_32x32x16_bf16 v[0:15], v[108:111], v[76:79], v[0:15]\n\t" \
    "ds_read_b64_tr_b16 v[140:141], v171 offset:32768\n\t" \
    "ds_read_b64_tr_b16 v[142:143], v171 offset:33792\n\t" \
    "ds_read_b64_tr_b16 v[156:157], v175 offset:32768\n\t" \
    "ds_read_b64_tr_b16 v[158:159], v175 offset:33792\n\t" \
    "v_lshlrev_b32 v52, 16, v99\n\t" \
    "v_and_b32 v53, v56, v99\n\t" \
    "v_lshlrev_b32 v54, 16, v115\n\t" \
    "v_and_b32 v55, v56, v115\n\t" \
    "v_mfma_f32_32x32x16_bf16 v[16:31], v[124:127], v[76:79], v[16:31]\n\t" \
    "v_add_f32 v48, v48, v52\n\t" \
    "v_add_f32 v49, v49, v53\n\t" \
    "v_add_f32 v50, v50, v54\n\t" \
    "v_add_f32 v51, v51, v55\n\t" \
    "s_cmp_lg_u32 %[wid], 0\n\t" \
    "s_cbranch_scc1 NOT00_%=\n\t" \
    "s_mov_b32 %[sx], 640\n\t" \
    "buffer_load_dword v57, v188, %[rxx], %[sx] offen\n\t" \
    "NOT00_%=:\n\t" \
    "v_mfma_f32_4x4x4_16b_bf16 v[32:35], v[64:65], v[64:65], v[32:35]\n\t" \
    "v_mfma_f32_4x4x4_16b_bf16 v[58:61], v[64:65], v[62:63], v[58:61]\n\t" \
    "v_mfma_f32_4x4x4_16b_bf16 v[36:39], v[66:67], v[66:67], v[36:39]\n\t" \
    "v_mfma_f32_4x4x4_16b_bf16 v[190:193], v[66:67], v[62:63], v[190:193]\n\t" \
    "v_mfma_f32_4x4x4_16b_bf16 v[40:43], v[68:69], v[68:69], v[40:43]\n\t" \
    "v_mfma_f32_4x4x4_16b_bf16 v[58:61], v[68:69], v[62:63], v[58:61]\n\t" \
    "v_mfma_f32_4x4x4_16b_bf16 v[44:47], v[70:71], v[70:71], v[44:47]\n\t" \
    "v_mfma_f32_4x4x4_16b_bf16 v[190:193], v[70:71], v[62:63], v[190:193]\n\t" \
    "s_waitcnt vmcnt(8) lgkmcnt(0)\n\t" \
    "s_barrier\n\t" \
    "v_mfma_f32_32x32x16_bf16 v[0:15], v[128:131], v[80:83], v[0:15]\n\t" \
    "s_add_u32 m0, %[mx], 32768\n\t" \
    "s_mov_b32 %[sx], 640\n\t" \
    "buffer_load_dwordx4 v184, %[rx], %[sx] offen lds\n\t" \
    "buffer_load_dwordx4 v185, %[rx], %[sx] offen offset:1024 lds\n\t" \
    "v_lshlrev_b32 v52, 16, v128\n\t" \
    "v_and_b32 v53, v56, v128\n\t" \
    "v_lshlrev_b32 v54, 16, v144\n\t" \
    "v_and_b32 v55, v56, v144\n\t" \
    "v_mfma_f32_32x32x16_bf16 v[16:31], v[144:147], v[80:83], v[16:31]\n\t" \
    "s_add_u32 m0, %[mx], 49152\n\t" \
    "s_mov_b32 %[sw], 1228800\n\t" \
    "buffer_load_dwordx4 v186, %[rw], %[sw] offen lds\n\t" \
    "buffer_load_dwordx4 v187, %[rw], %[sw] offen offset:1024 lds\n\t" \
    "v_add_f32 v48, v48, v52\n\t" \
    "v_add_f32 v49, v49, v53\n\t" \
    "v_add_f32 v50, v50, v54\n\t" \
    "v_add_f32 v51, v51, v55\n\t" \
    "v_mfma_f32_32x32x16_bf16 v[0:15], v[132:135], v[84:87], v[0:15]\n\t" \
    "ds_read_b128 v[64:67], v164 offset:0\n\t" \
    "ds_read_b64_tr_b16 v[96:97], v176 offset:0\n\t" \
    "ds_read_b64_tr_b16 v[98:99], v176 offset:1024\n\t" \
    "ds_read_b64_tr_b16 v[112:113], v180 offset:0\n\t" \
    "v_lshlrev_b32 v52, 16, v129\n\t" \
    "v_and_b32 v53, v56, v129\n\t" \
    "v_lshlrev_b32 v54, 16, v145\n\t" \
    "v_and_b32 v55, v56, v145\n\t" \
    "v_mfma_f32_32x32x16_bf16 v[16:31], v[148:151], v[84:87], v[16:31]\n\t" \
    "ds_read_b64_tr_b16 v[114:115], v180 offset:1024\n\t" \
    "ds_read_b128 v[68:71], v165 offset:0\n\t" \
    "ds_read_b64_tr_b16 v[100:101], v177 offset:0\n\t" \
    "ds_read_b64_tr_b16 v[102:103], v177 offset:1024\n\t" \
    "v_add_f32 v48, v48, v52\n\t" \
    "v_add_f32 v49, v49, v53\n\t" \
    "v_add_f32 v50, v50, v54\n\t" \
    "v_add_f32 v51, v51, v55\n\t" \
    "v_mfma_f32_32x32x16_bf16 v[0:15], v[136:139], v[88:91], v[0:15]\n\t" \
    "ds_read_b64_tr_b16 v[116:117], v181 offset:0\n\t" \
    "ds_read_b64_tr_b16 v[118:119], v181 offset:1024\n\t" \
    "ds_read_b128 v[72:75], v166 offset:0\n\t" \
    "ds_read_b64_tr_b16 v[104:105], v178 offset:0\n\t" \
    "v_lshlrev_b32 v52, 16, v130\n\t" \
    "v_and_b32 v53, v56, v130\n\t" \
    "v_lshlrev_b32 v54, 16, v146\n\t" \
    "v_and_b32 v55, v56, v146\n\t" \
    "v_mfma_f32_32x32x16_bf16 v[16:31], v[152:155], v[88:91], v[16:31]\n\t" \
    "ds_read_b64_tr_b16 v[106:107], v178 offset:1024\n\t" \
    "ds_read_b64_tr_b16 v[120:121], v182 offset:0\n\t" \
    "ds_read_b64_tr_b16 v[122:123], v182 offset:1024\n\t" \
    "ds_read_b128 v[76:79], v167 offset:0\n\t" \
    "v_add_f32 v48, v48, v52\n\t" \
    "v_add_f32 v49, v49, v53\n\t" \
    "v_add_f32 v50, v50, v54\n\t" \
    "v_add_f32 v51, v51, v55\n\t" \
    "v_mfma_f32_32x32x16_bf16 v[0:15], v[140:143], v[92:95], v[0:15]\n\t" \
    "ds_read_b64_tr_b16 v[108:109], v179 offset:0\n\t" \
    "ds_read_b64_tr_b16 v[110:111], v179 offset:1024\n\t" \
    "ds_read_b64_tr_b16 v[124:125], v183 offset:0\n\t" \
    "ds_read_b64_tr_b16 v[126:127], v183 offset:1024\n\t" \
    "v_lshlrev_b32 v52, 16, v131\n\t" \
    "v_and_b32 v53, v56, v131\n\t" \
    "v_lshlrev_b32 v54, 16, v147\n\t" \
    "v_and_b32 v55, v56, v147\n\t" \
    "v_mfma_f32_32x32x16_bf16 v[16:31], v[156:159], v[92:95], v[16:31]\n\t" \
    "v_add_f32 v48, v48, v52\n\t" \
    "v_add_f32 v49, v49, v53\n\t" \
    "v_add_f32 v50, v50, v54\n\t" \
    "v_add_f32 v51, v51, v55\n\t" \
    "s_cmp_lg_u32 %[wid], 1\n\t" \
    "s_cbranch_scc1 NOT10_%=\n\t" \
    "s_mov_b32 %[sx], 1474560\n\t" \
    "buffer_load_dword v57, v189, %[rw], %[sx] offen\n\t" \
    "NOT10_%=:\n\t" \
    "v_mfma_f32_4x4x4_16b_bf16 v[32:35], v[80:81], v[80:81], v[32:35]\n\t" \
    "v_mfma_f32_4x4x4_16b_bf16 v[58:61], v[80:81], v[62:63], v[58:61]\n\t" \
    "v_mfma_f32_4x4x4_16b_bf16 v[36:39], v[82:83], v[82:83], v[36:39]\n\t" \
    "v_mfma_f32_4x4x4_16b_bf16 v[190:193], v[82:83], v[62:63], v[190:193]\n\t" \
    "v_mfma_f32_4x4x4_16b_bf16 v[40:43], v[84:85], v[84:85], v[40:43]\n\t" \
    "v_mfma_f32_4x4x4_16b_bf16 v[58:61], v[84:85], v[62:63], v[58:61]\n\t" \
    "v_mfma_f32_4x4x4_16b_bf16 v[44:47], v[86:87], v[86:87], v[44:47]\n\t" \
    "v_mfma_f32_4x4x4_16b_bf16 v[190:193], v[86:87], v[62:63], v[190:193]\n\t" \
    "s_waitcnt vmcnt(8) lgkmcnt(0)\n\t" \
    "s_barrier\n\t" \
    "v_mfma_f32_32x32x16_bf16 v[0:15], v[96:99], v[64:67], v[0:15]\n\t" \
    "s_add_u32 m0, %[mx], 65536\n\t" \
    "s_mov_b32 %[sx], 768\n\t" \
    "buffer_load_dwordx4 v184, %[rx], %[sx] offen lds\n\t" \
    "buffer_load_dwordx4 v185, %[rx], %[sx] offen offset:1024 lds\n\t" \
    "v_lshlrev_b32 v52, 16, v96\n\t" \
    "v_and_b32 v53, v56, v96\n\t" \
    "v_lshlrev_b32 v54, 16, v112\n\t" \
    "v_and_b32 v55, v56, v112\n\t" \
    "v_mfma_f32_32x32x16_bf16 v[16:31], v[112:115], v[64:67], v[16:31]\n\t" \
    "s_add_u32 m0, %[mx], 81920\n\t" \
    "s_mov_b32 %[sw], 1474560\n\t" \
    "buffer_load_dwordx4 v186, %[rw], %[sw] offen lds\n\t" \
    "buffer_load_dwordx4 v187, %[rw], %[sw] offen offset:1024 lds\n\t" \
    "v_add_f32 v48, v48, v52\n\t" \
    "v_add_f32 v49, v49, v53\n\t" \
    "v_add_f32 v50, v50, v54\n\t" \
    "v_add_f32 v51, v51, v55\n\t" \
    "v_mfma_f32_32x32x16_bf16 v[0:15], v[100:103], v[68:71], v[0:15]\n\t" \
    "ds_read_b128 v[80:83], v164 offset:32768\n\t" \
    "ds_read_b64_tr_b16 v[128:129], v176 offset:32768\n\t" \
    "ds_read_b64_tr_b16 v[130:131], v176 offset:33792\n\t" \
    "ds_read_b64_tr_b16 v[144:145], v180 offset:32768\n\t" \
    "v_lshlrev_b32 v52, 16, v97\n\t" \
    "v_and_b32 v53, v56, v97\n\t" \
    "v_lshlrev_b32 v54, 16, v113\n\t" \
    "v_and_b32 v55, v56, v113\n\t" \
    "v_mfma_f32_32x32x16_bf16 v[16:31], v[116:119], v[68:71], v[16:31]\n\t" \
    "ds_read_b64_tr_b16 v[146:147], v180 offset:33792\n\t" \
    "ds_read_b128 v[84:87], v165 offset:32768\n\t" \
    "ds_read_b64_tr_b16 v[132:133], v177 offset:32768\n\t" \
    "ds_read_b64_tr_b16 v[134:135], v177 offset:33792\n\t" \
    "v_add_f32 v48, v48, v52\n\t" \
    "v_add_f32 v49, v49, v53\n\t" \
    "v_add_f32 v50, v50, v54\n\t" \
    "v_add_f32 v51, v51, v55\n\t" \
    "v_mfma_f32_32x32x16_bf16 v[0:15], v[104:107], v[72:75], v[0:15]\n\t" \
    "ds_read_b64_tr_b16 v[148:149], v181 offset:32768\n\t" \
    "ds_read_b64_tr_b16 v[150:151], v181 offset:33792\n\t" \
    "ds_read_b128 v[88:91], v166 offset:32768\n\t" \
    "ds_read_b64_tr_b16 v[136:137], v178 offset:32768\n\t" \
    "v_lshlrev_b32 v52, 16, v98\n\t" \
    "v_and_b32 v53, v56, v98\n\t" \
    "v_lshlrev_b32 v54, 16, v114\n\t" \
    "v_and_b32 v55, v56, v114\n\t" \
    "v_mfma_f32_32x32x16_bf16 v[16:31], v[120:123], v[72:75], v[16:31]\n\t" \
    "ds_read_b64_tr_b16 v[138:139], v178 offset:33792\n\t" \
    "ds_read_b64_tr_b16 v[152:153], v182 offset:32768\n\t" \
    "ds_read_b64_tr_b16 v[154:155], v182 offset:33792\n\t" \
    "ds_read_b128 v[92:95], v167 offset:32768\n\t" \
    "v_add_f32 v48, v48, v52\n\t" \
    "v_add_f32 v49, v49, v53\n\t" \
    "v_add_f32 v50, v50, v54\n\t" \
    "v_add_f32 v51, v51, v55\n\t" \
    "v_mfma_f32_32x32x16_bf16 v[0:15], v[108:111], v[76:79], v[0:15]\n\t" \
    "ds_read_b64_tr_b16 v[140:141], v179 offset:32768\n\t" \
    "ds_read_b64_tr_b16 v[142:143], v179 offset:33792\n\t" \
    "ds_read_b64_tr_b16 v[156:157], v183 offset:32768\n\t" \
    "ds_read_b64_tr_b16 v[158:159], v183 offset:33792\n\t" \
    "v_lshlrev_b32 v52, 16, v99\n\t" \
    "v_and_b32 v53, v56, v99\n\t" \
    "v_lshlrev_b32 v54, 16, v115\n\t" \
    "v_and_b32 v55, v56, v115\n\t" \
    "v_mfma_f32_32x32x16_bf16 v[16:31], v[124:127], v[76:79], v[16:31]\n\t" \
    "v_add_f32 v48, v48, v52\n\t" \
    "v_add_f32 v49, v49, v53\n\t" \
    "v_add_f32 v50, v50, v54\n\t" \
    "v_add_f32 v51, v51, v55\n\t" \
    "s_cmp_lg_u32 %[wid], 2\n\t" \
    "s_cbranch_scc1 NOT20_%=\n\t" \
    "s_mov_b32 %[sx], 896\n\t" \
    "buffer_load_dword v57, v188, %[rxx], %[sx] offen\n\t" \
    "NOT20_%=:\n\t" \
    "v_mfma_f32_4x4x4_16b_bf16 v[32:35], v[64:65], v[64:65], v[32:35]\n\t" \
    "v_mfma_f32_4x4x4_16b_bf16 v[58:61], v[64:65], v[62:63], v[58:61]\n\t" \
    "v_mfma_f32_4x4x4_16b_bf16 v[36:39], v[66:67], v[66:67], v[36:39]\n\t" \
    "v_mfma_f32_4x4x4_16b_bf16 v[190:193], v[66:67], v[62:63], v[190:193]\n\t" \
    "v_mfma_f32_4x4x4_16b_bf16 v[40:43], v[68:69], v[68:69], v[40:43]\n\t" \
    "v_mfma_f32_4x4x4_16b_bf16 v[58:61], v[68:69], v[62:63], v[58:61]\n\t" \
    "v_mfma_f32_4x4x4_16b_bf16 v[44:47], v[70:71], v[70:71], v[44:47]\n\t" \
    "v_mfma_f32_4x4x4_16b_bf16 v[190:193], v[70:71], v[62:63], v[190:193]\n\t" \
    "s_waitcnt vmcnt(8) lgkmcnt(0)\n\t" \
    "s_barrier\n\t" \
    "v_mfma_f32_32x32x16_bf16 v[0:15], v[128:131], v[80:83], v[0:15]\n\t" \
    "s_add_u32 m0, %[mx], 98304\n\t" \
    "s_mov_b32 %[sx], 896\n\t" \
    "buffer_load_dwordx4 v184, %[rx], %[sx] offen lds\n\t" \
    "buffer_load_dwordx4 v185, %[rx], %[sx] offen offset:1024 lds\n\t" \
    "v_lshlrev_b32 v52, 16, v128\n\t" \
    "v_and_b32 v53, v56, v128\n\t" \
    "v_lshlrev_b32 v54, 16, v144\n\t" \
    "v_and_b32 v55, v56, v144\n\t" \
    "v_mfma_f32_32x32x16_bf16 v[16:31], v[144:147], v[80:83], v[16:31]\n\t" \
    "s_add_u32 m0, %[mx], 114688\n\t" \
    "s_mov_b32 %[sw], 1720320\n\t" \
    "buffer_load_dwordx4 v186, %[rw], %[sw] offen lds\n\t" \
    "buffer_load_dwordx4 v187, %[rw], %[sw] offen offset:1024 lds\n\t" \
    "v_add_f32 v48, v48, v52\n\t" \
    "v_add_f32 v49, v49, v53\n\t" \
    "v_add_f32 v50, v50, v54\n\t" \
    "v_add_f32 v51, v51, v55\n\t" \
    "v_mfma_f32_32x32x16_bf16 v[0:15], v[132:135], v[84:87], v[0:15]\n\t" \
    "ds_read_b128 v[64:67], v160 offset:0\n\t" \
    "ds_read_b64_tr_b16 v[96:97], v168 offset:0\n\t" \
    "ds_read_b64_tr_b16 v[98:99], v168 offset:1024\n\t" \
    "ds_read_b64_tr_b16 v[112:113], v172 offset:0\n\t" \
    "v_lshlrev_b32 v52, 16, v129\n\t" \
    "v_and_b32 v53, v56, v129\n\t" \
    "v_lshlrev_b32 v54, 16, v145\n\t" \
    "v_and_b32 v55, v56, v145\n\t" \
    "v_mfma_f32_32x32x16_bf16 v[16:31], v[148:151], v[84:87], v[16:31]\n\t" \
    "ds_read_b64_tr_b16 v[114:115], v172 offset:1024\n\t" \
    "ds_read_b128 v[68:71], v161 offset:0\n\t" \
    "ds_read_b64_tr_b16 v[100:101], v169 offset:0\n\t" \
    "ds_read_b64_tr_b16 v[102:103], v169 offset:1024\n\t" \
    "v_add_f32 v48, v48, v52\n\t" \
    "v_add_f32 v49, v49, v53\n\t" \
    "v_add_f32 v50, v50, v54\n\t" \
    "v_add_f32 v51, v51, v55\n\t" \
    "v_mfma_f32_32x32x16_bf16 v[0:15], v[136:139], v[88:91], v[0:15]\n\t" \
    "ds_read_b64_tr_b16 v[116:117], v173 offset:0\n\t" \
    "ds_read_b64_tr_b16 v[118:119], v173 offset:1024\n\t" \
    "ds_read_b128 v[72:75], v162 offset:0\n\t" \
    "ds_read_b64_tr_b16 v[104:105], v170 offset:0\n\t" \
    "v_lshlrev_b32 v52, 16, v130\n\t" \
    "v_and_b32 v53, v56, v130\n\t" \
    "v_lshlrev_b32 v54, 16, v146\n\t" \
    "v_and_b32 v55, v56, v146\n\t" \
    "v_mfma_f32_32x32x16_bf16 v[16:31], v[152:155], v[88:91], v[16:31]\n\t" \
    "ds_read_b64_tr_b16 v[106:107], v170 offset:1024\n\t" \
    "ds_read_b64_tr_b16 v[120:121], v174 offset:0\n\t" \
    "ds_read_b64_tr_b16 v[122:123], v174 offset:1024\n\t" \
    "ds_read_b128 v[76:79], v163 offset:0\n\t" \
    "v_add_f32 v48, v48, v52\n\t" \
    "v_add_f32 v49, v49, v53\n\t" \
    "v_add_f32 v50, v50, v54\n\t" \
    "v_add_f32 v51, v51, v55\n\t" \
    "v_mfma_f32_32x32x16_bf16 v[0:15], v[140:143], v[92:95], v[0:15]\n\t" \
    "ds_read_b64_tr_b16 v[108:109], v171 offset:0\n\t" \
    "ds_read_b64_tr_b16 v[110:111], v171 offset:1024\n\t" \
    "ds_read_b64_tr_b16 v[124:125], v175 offset:0\n\t" \
    "ds_read_b64_tr_b16 v[126:127], v175 offset:1024\n\t" \
    "v_lshlrev_b32 v52, 16, v131\n\t" \
    "v_and_b32 v53, v56, v131\n\t" \
    "v_lshlrev_b32 v54, 16, v147\n\t" \
    "v_and_b32 v55, v56, v147\n\t" \
    "v_mfma_f32_32x32x16_bf16 v[16:31], v[156:159], v[92:95], v[16:31]\n\t" \
    "v_add_f32 v48, v48, v52\n\t" \
    "v_add_f32 v49, v49, v53\n\t" \
    "v_add_f32 v50, v50, v54\n\t" \
    "v_add_f32 v51, v51, v55\n\t" \
    "s_cmp_lg_u32 %[wid], 3\n\t" \
    "s_cbranch_scc1 NOT30_%=\n\t" \
    "s_mov_b32 %[sx], 2211840\n\t" \
    "buffer_load_dword v57, v189, %[rw], %[sx] offen\n\t" \
    "NOT30_%=:\n\t" \
    "s_cmp_lg_u32 %[wid], 6\n\t" \
    "s_cbranch_scc1 NOT31_%=\n\t" \
    "s_mov_b32 %[sx], 1152\n\t" \
    "buffer_load_dword v57, v188, %[rxx], %[sx] offen\n\t" \
    "NOT31_%=:\n\t" \
    "v_mfma_f32_4x4x4_16b_bf16 v[32:35], v[80:81], v[80:81], v[32:35]\n\t" \
    "v_mfma_f32_4x4x4_16b_bf16 v[58:61], v[80:81], v[62:63], v[58:61]\n\t" \
    "v_mfma_f32_4x4x4_16b_bf16 v[36:39], v[82:83], v[82:83], v[36:39]\n\t" \
    "v_mfma_f32_4x4x4_16b_bf16 v[190:193], v[82:83], v[62:63], v[190:193]\n\t" \
    "v_mfma_f32_4x4x4_16b_bf16 v[40:43], v[84:85], v[84:85], v[40:43]\n\t" \
    "v_mfma_f32_4x4x4_16b_bf16 v[58:61], v[84:85], v[62:63], v[58:61]\n\t" \
    "v_mfma_f32_4x4x4_16b_bf16 v[44:47], v[86:87], v[86:87], v[44:47]\n\t" \
    "v_mfma_f32_4x4x4_16b_bf16 v[190:193], v[86:87], v[62:63], v[190:193]\n\t" \
    "s_waitcnt vmcnt(8) lgkmcnt(0)\n\t" \
    "s_barrier\n\t" \
    "v_mfma_f32_32x32x16_bf16 v[0:15], v[96:99], v[64:67], v[0:15]\n\t" \
    "s_add_u32 m0, %[mx], 0\n\t" \
    "s_mov_b32 %[sx], 1024\n\t" \
    "buffer_load_dwordx4 v184, %[rx], %[sx] offen lds\n\t" \
    "buffer_load_dwordx4 v185, %[rx], %[sx] offen offset:1024 lds\n\t" \
    "v_lshlrev_b32 v52, 16, v96\n\t" \
    "v_and_b32 v53, v56, v96\n\t" \
    "v_lshlrev_b32 v54, 16, v112\n\t" \
    "v_and_b32 v55, v56, v112\n\t" \
    "v_mfma_f32_32x32x16_bf16 v[16:31], v[112:115], v[64:67], v[16:31]\n\t" \
    "s_add_u32 m0, %[mx], 16384\n\t" \
    "s_mov_b32 %[sw], 1966080\n\t" \
    "buffer_load_dwordx4 v186, %[rw], %[sw] offen lds\n\t" \
    "buffer_load_dwordx4 v187, %[rw], %[sw] offen offset:1024 lds\n\t" \
    "v_add_f32 v48, v48, v52\n\t" \
    "v_add_f32 v49, v49, v53\n\t" \
    "v_add_f32 v50, v50, v54\n\t" \
    "v_add_f32 v51, v51, v55\n\t" \
    "v_mfma_f32_32x32x16_bf16 v[0:15], v[100:103], v[68:71], v[0:15]\n\t" \
    "ds_read_b128 v[80:83], v160 offset:32768\n\t" \
    "ds_read_b64_tr_b16 v[128:129], v168 offset:32768\n\t" \
    "ds_read_b64_tr_b16 v[130:131], v168 offset:33792\n\t" \
    "ds_read_b64_tr_b16 v[144:145], v172 offset:32768\n\t" \
    "v_lshlrev_b32 v52, 16, v97\n\t" \
    "v_and_b32 v53, v56, v97\n\t" \
    "v_lshlrev_b32 v54, 16, v113\n\t" \
    "v_and_b32 v55, v56, v113\n\t" \
    "v_mfma_f32_32x32x16_bf16 v[16:31], v[116:119], v[68:71], v[16:31]\n\t" \
    "ds_read_b64_tr_b16 v[146:147], v172 offset:33792\n\t" \
    "ds_read_b128 v[84:87], v161 offset:32768\n\t" \
    "ds_read_b64_tr_b16 v[132:133], v169 offset:32768\n\t" \
    "ds_read_b64_tr_b16 v[134:135], v169 offset:33792\n\t" \
    "v_add_f32 v48, v48, v52\n\t" \
    "v_add_f32 v49, v49, v53\n\t" \
    "v_add_f32 v50, v50, v54\n\t" \
    "v_add_f32 v51, v51, v55\n\t" \
    "v_mfma_f32_32x32x16_bf16 v[0:15], v[104:107], v[72:75], v[0:15]\n\t" \
    "ds_read_b64_tr_b16 v[148:149], v173 offset:32768\n\t" \
    "ds_read_b64_tr_b16 v[150:151], v173 offset:33792\n\t" \
    "ds_read_b128 v[88:91], v162 offset:32768\n\t" \
    "ds_read_b64_tr_b16 v[136:137], v170 offset:32768\n\t" \
    "v_lshlrev_b32 v52, 16, v98\n\t" \
    "v_and_b32 v53, v56, v98\n\t" \
    "v_lshlrev_b32 v54, 16, v114\n\t" \
    "v_and_b32 v55, v56, v114\n\t" \
    "v_mfma_f32_32x32x16_bf16 v[16:31], v[120:123], v[72:75], v[16:31]\n\t" \
    "ds_read_b64_tr_b16 v[138:139], v170 offset:33792\n\t" \
    "ds_read_b64_tr_b16 v[152:153], v174 offset:32768\n\t" \
    "ds_read_b64_tr_b16 v[154:155], v174 offset:33792\n\t" \
    "ds_read_b128 v[92:95], v163 offset:32768\n\t" \
    "v_add_f32 v48, v48, v52\n\t" \
    "v_add_f32 v49, v49, v53\n\t" \
    "v_add_f32 v50, v50, v54\n\t" \
    "v_add_f32 v51, v51, v55\n\t" \
    "v_mfma_f32_32x32x16_bf16 v[0:15], v[108:111], v[76:79], v[0:15]\n\t" \
    "ds_read_b64_tr_b16 v[140:141], v171 offset:32768\n\t" \
    "ds_read_b64_tr_b16 v[142:143], v171 offset:33792\n\t" \
    "ds_read_b64_tr_b16 v[156:157], v175 offset:32768\n\t" \
    "ds_read_b64_tr_b16 v[158:159], v175 offset:33792\n\t" \
    "v_lshlrev_b32 v52, 16, v99\n\t" \
    "v_and_b32 v53, v56, v99\n\t" \
    "v_lshlrev_b32 v54, 16, v115\n\t" \
    "v_and_b32 v55, v56, v115\n\t" \
    "v_mfma_f32_32x32x16_bf16 v[16:31], v[124:127], v[76:79], v[16:31]\n\t" \
    "v_add_f32 v48, v48, v52\n\t" \
    "v_add_f32 v49, v49, v53\n\t" \
    "v_add_f32 v50, v50, v54\n\t" \
    "v_add_f32 v51, v51, v55\n\t" \
    "v_mfma_f32_4x4x4_16b_bf16 v[32:35], v[64:65], v[64:65], v[32:35]\n\t" \
    "v_mfma_f32_4x4x4_16b_bf16 v[58:61], v[64:65], v[62:63], v[58:61]\n\t" \
    "v_mfma_f32_4x4x4_16b_bf16 v[36:39], v[66:67], v[66:67], v[36:39]\n\t" \
    "v_mfma_f32_4x4x4_16b_bf16 v[190:193], v[66:67], v[62:63], v[190:193]\n\t" \
    "v_mfma_f32_4x4x4_16b_bf16 v[40:43], v[68:69], v[68:69], v[40:43]\n\t" \
    "v_mfma_f32_4x4x4_16b_bf16 v[58:61], v[68:69], v[62:63], v[58:61]\n\t" \
    "v_mfma_f32_4x4x4_16b_bf16 v[44:47], v[70:71], v[70:71], v[44:47]\n\t" \
    "v_mfma_f32_4x4x4_16b_bf16 v[190:193], v[70:71], v[62:63], v[190:193]\n\t" \
    "s_waitcnt vmcnt(8) lgkmcnt(0)\n\t" \
    "s_barrier\n\t" \
    "v_mfma_f32_32x32x16_bf16 v[0:15], v[128:131], v[80:83], v[0:15]\n\t" \
    "s_add_u32 m0, %[mx], 32768\n\t" \
    "s_mov_b32 %[sx], 1152\n\t" \
    "buffer_load_dwordx4 v184, %[rx], %[sx] offen lds\n\t" \
    "buffer_load_dwordx4 v185, %[rx], %[sx] offen offset:1024 lds\n\t" \
    "v_lshlrev_b32 v52, 16, v128\n\t" \
    "v_and_b32 v53, v56, v128\n\t" \
    "v_lshlrev_b32 v54, 16, v144\n\t" \
    "v_and_b32 v55, v56, v144\n\t" \
    "v_mfma_f32_32x32x16_bf16 v[16:31], v[144:147], v[80:83], v[16:31]\n\t" \
    "s_add_u32 m0, %[mx], 49152\n\t" \
    "s_mov_b32 %[sw], 2211840\n\t" \
    "buffer_load_dwordx4 v186, %[rw], %[sw] offen lds\n\t" \
    "buffer_load_dwordx4 v187, %[rw], %[sw] offen offset:1024 lds\n\t" \
    "v_add_f32 v48, v48, v52\n\t" \
    "v_add_f32 v49, v49, v53\n\t" \
    "v_add_f32 v50, v50, v54\n\t" \
    "v_add_f32 v51, v51, v55\n\t" \
    "v_mfma_f32_32x32x16_bf16 v[0:15], v[132:135], v[84:87], v[0:15]\n\t" \
    "ds_read_b128 v[64:67], v164 offset:0\n\t" \
    "ds_read_b64_tr_b16 v[96:97], v176 offset:0\n\t" \
    "ds_read_b64_tr_b16 v[98:99], v176 offset:1024\n\t" \
    "ds_read_b64_tr_b16 v[112:113], v180 offset:0\n\t" \
    "v_lshlrev_b32 v52, 16, v129\n\t" \
    "v_and_b32 v53, v56, v129\n\t" \
    "v_lshlrev_b32 v54, 16, v145\n\t" \
    "v_and_b32 v55, v56, v145\n\t" \
    "v_mfma_f32_32x32x16_bf16 v[16:31], v[148:151], v[84:87], v[16:31]\n\t" \
    "ds_read_b64_tr_b16 v[114:115], v180 offset:1024\n\t" \
    "ds_read_b128 v[68:71], v165 offset:0\n\t" \
    "ds_read_b64_tr_b16 v[100:101], v177 offset:0\n\t" \
    "ds_read_b64_tr_b16 v[102:103], v177 offset:1024\n\t" \
    "v_add_f32 v48, v48, v52\n\t" \
    "v_add_f32 v49, v49, v53\n\t" \
    "v_add_f32 v50, v50, v54\n\t" \
    "v_add_f32 v51, v51, v55\n\t" \
    "v_mfma_f32_32x32x16_bf16 v[0:15], v[136:139], v[88:91], v[0:15]\n\t" \
    "ds_read_b64_tr_b16 v[116:117], v181 offset:0\n\t" \
    "ds_read_b64_tr_b16 v[118:119], v181 offset:1024\n\t" \
    "ds_read_b128 v[72:75], v166 offset:0\n\t" \
    "ds_read_b64_tr_b16 v[104:105], v178 offset:0\n\t" \
    "v_lshlrev_b32 v52, 16, v130\n\t" \
    "v_and_b32 v53, v56, v130\n\t" \
    "v_lshlrev_b32 v54, 16, v146\n\t" \
    "v_and_b32 v55, v56, v146\n\t" \
    "v_mfma_f32_32x32x16_bf16 v[16:31], v[152:155], v[88:91], v[16:31]\n\t" \
    "ds_read_b64_tr_b16 v[106:107], v178 offset:1024\n\t" \
    "ds_read_b64_tr_b16 v[120:121], v182 offset:0\n\t" \
    "ds_read_b64_tr_b16 v[122:123], v182 offset:1024\n\t" \
    "ds_read_b128 v[76:79], v167 offset:0\n\t" \
    "v_add_f32 v48, v48, v52\n\t" \
    "v_add_f32 v49, v49, v53\n\t" \
    "v_add_f32 v50, v50, v54\n\t" \
    "v_add_f32 v51, v51, v55\n\t" \
    "v_mfma_f32_32x32x16_bf16 v[0:15], v[140:143], v[92:95], v[0:15]\n\t" \
    "ds_read_b64_tr_b16 v[108:109], v179 offset:0\n\t" \
    "ds_read_b64_tr_b16 v[110:111], v179 offset:1024\n\t" \
    "ds_read_b64_tr_b16 v[124:125], v183 offset:0\n\t" \
    "ds_read_b64_tr_b16 v[126:127], v183 offset:1024\n\t" \
    "v_lshlrev_b32 v52, 16, v131\n\t" \
    "v_and_b32 v53, v56, v131\n\t" \
    "v_lshlrev_b32 v54, 16, v147\n\t" \
    "v_and_b32 v55, v56, v147\n\t" \
    "v_mfma_f32_32x32x16_bf16 v[16:31], v[156:159], v[92:95], v[16:31]\n\t" \
    "v_add_f32 v48, v48, v52\n\t" \
    "v_add_f32 v49, v49, v53\n\t" \
    "v_add_f32 v50, v50, v54\n\t" \
    "v_add_f32 v51, v51, v55\n\t" \
    "v_mfma_f32_4x4x4_16b_bf16 v[32:35], v[80:81], v[80:81], v[32:35]\n\t" \
    "v_mfma_f32_4x4x4_16b_bf16 v[58:61], v[80:81], v[62:63], v[58:61]\n\t" \
    "v_mfma_f32_4x4x4_16b_bf16 v[36:39], v[82:83], v[82:83], v[36:39]\n\t" \
    "v_mfma_f32_4x4x4_16b_bf16 v[190:193], v[82:83], v[62:63], v[190:193]\n\t" \
    "v_mfma_f32_4x4x4_16b_bf16 v[40:43], v[84:85], v[84:85], v[40:43]\n\t" \
    "v_mfma_f32_4x4x4_16b_bf16 v[58:61], v[84:85], v[62:63], v[58:61]\n\t" \
    "v_mfma_f32_4x4x4_16b_bf16 v[44:47], v[86:87], v[86:87], v[44:47]\n\t" \
    "v_mfma_f32_4x4x4_16b_bf16 v[190:193], v[86:87], v[62:63], v[190:193]\n\t" \
    "s_waitcnt vmcnt(8) lgkmcnt(0)\n\t" \
    "s_barrier\n\t" \
    "v_mfma_f32_32x32x16_bf16 v[0:15], v[96:99], v[64:67], v[0:15]\n\t" \
    "ds_read_b128 v[80:83], v164 offset:32768\n\t" \
    "ds_read_b64_tr_b16 v[128:129], v176 offset:32768\n\t" \
    "ds_read_b64_tr_b16 v[130:131], v176 offset:33792\n\t" \
    "v_lshlrev_b32 v52, 16, v96\n\t" \
    "v_and_b32 v53, v56, v96\n\t" \
    "v_lshlrev_b32 v54, 16, v112\n\t" \
    "v_and_b32 v55, v56, v112\n\t" \
    "v_mfma_f32_32x32x16_bf16 v[16:31], v[112:115], v[64:67], v[16:31]\n\t" \
    "ds_read_b64_tr_b16 v[144:145], v180 offset:32768\n\t" \
    "ds_read_b64_tr_b16 v[146:147], v180 offset:33792\n\t" \
    "ds_read_b128 v[84:87], v165 offset:32768\n\t" \
    "v_add_f32 v48, v48, v52\n\t" \
    "v_add_f32 v49, v49, v53\n\t" \
    "v_add_f32 v50, v50, v54\n\t" \
    "v_add_f32 v51, v51, v55\n\t" \
    "v_mfma_f32_32x32x16_bf16 v[0:15], v[100:103], v[68:71], v[0:15]\n\t" \
    "ds_read_b64_tr_b16 v[132:133], v177 offset:32768\n\t" \
    "ds_read_b64_tr_b16 v[134:135], v177 offset:33792\n\t" \
    "ds_read_b64_tr_b16 v[148:149], v181 offset:32768\n\t" \
    "v_lshlrev_b32 v52, 16, v97\n\t" \
    "v_and_b32 v53, v56, v97\n\t" \
    "v_lshlrev_b32 v54, 16, v113\n\t" \
    "v_and_b32 v55, v56, v113\n\t" \
    "v_mfma_f32_32x32x16_bf16 v[16:31], v[116:119], v[68:71], v[16:31]\n\t" \
    "ds_read_b64_tr_b16 v[150:151], v181 offset:33792\n\t" \
    "ds_read_b128 v[88:91], v166 offset:32768\n\t" \
    "ds_read_b64_tr_b16 v[136:137], v178 offset:32768\n\t" \
    "v_add_f32 v48, v48, v52\n\t" \
    "v_add_f32 v49, v49, v53\n\t" \
    "v_add_f32 v50, v50, v54\n\t" \
    "v_add_f32 v51, v51, v55\n\t" \
    "v_mfma_f32_32x32x16_bf16 v[0:15], v[104:107], v[72:75], v[0:15]\n\t" \
    "ds_read_b64_tr_b16 v[138:139], v178 offset:33792\n\t" \
    "ds_read_b64_tr_b16 v[152:153], v182 offset:32768\n\t" \
    "ds_read_b64_tr_b16 v[154:155], v182 offset:33792\n\t" \
    "v_lshlrev_b32 v52, 16, v98\n\t" \
    "v_and_b32 v53, v56, v98\n\t" \
    "v_lshlrev_b32 v54, 16, v114\n\t" \
    "v_and_b32 v55, v56, v114\n\t" \
    "v_mfma_f32_32x32x16_bf16 v[16:31], v[120:123], v[72:75], v[16:31]\n\t" \
    "ds_read_b128 v[92:95], v167 offset:32768\n\t" \
    "ds_read_b64_tr_b16 v[140:141], v179 offset:32768\n\t" \
    "ds_read_b64_tr_b16 v[142:143], v179 offset:33792\n\t" \
    "v_add_f32 v48, v48, v52\n\t" \
    "v_add_f32 v49, v49, v53\n\t" \
    "v_add_f32 v50, v50, v54\n\t" \
    "v_add_f32 v51, v51, v55\n\t" \
    "v_mfma_f32_32x32x16_bf16 v[0:15], v[108:111], v[76:79], v[0:15]\n\t" \
    "ds_read_b64_tr_b16 v[156:157], v183 offset:32768\n\t" \
    "ds_read_b64_tr_b16 v[158:159], v183 offset:33792\n\t" \
    "v_lshlrev_b32 v52, 16, v99\n\t" \
    "v_and_b32 v53, v56, v99\n\t" \
    "v_lshlrev_b32 v54, 16, v115\n\t" \
    "v_and_b32 v55, v56, v115\n\t" \
    "v_mfma_f32_32x32x16_bf16 v[16:31], v[124:127], v[76:79], v[16:31]\n\t" \
    "v_add_f32 v48, v48, v52\n\t" \
    "v_add_f32 v49, v49, v53\n\t" \
    "v_add_f32 v50, v50, v54\n\t" \
    "v_add_f32 v51, v51, v55\n\t" \
    "v_mfma_f32_4x4x4_16b_bf16 v[32:35], v[64:65], v[64:65], v[32:35]\n\t" \
    "v_mfma_f32_4x4x4_16b_bf16 v[58:61], v[64:65], v[62:63], v[58:61]\n\t" \
    "v_mfma_f32_4x4x4_16b_bf16 v[36:39], v[66:67], v[66:67], v[36:39]\n\t" \
    "v_mfma_f32_4x4x4_16b_bf16 v[190:193], v[66:67], v[62:63], v[190:193]\n\t" \
    "v_mfma_f32_4x4x4_16b_bf16 v[40:43], v[68:69], v[68:69], v[40:43]\n\t" \
    "v_mfma_f32_4x4x4_16b_bf16 v[58:61], v[68:69], v[62:63], v[58:61]\n\t" \
    "v_mfma_f32_4x4x4_16b_bf16 v[44:47], v[70:71], v[70:71], v[44:47]\n\t" \
    "v_mfma_f32_4x4x4_16b_bf16 v[190:193], v[70:71], v[62:63], v[190:193]\n\t" \
    "s_waitcnt vmcnt(4) lgkmcnt(0)\n\t" \
    "s_barrier\n\t" \
    "v_mfma_f32_32x32x16_bf16 v[0:15], v[128:131], v[80:83], v[0:15]\n\t" \
    "ds_read_b128 v[64:67], v160 offset:0\n\t" \
    "ds_read_b64_tr_b16 v[96:97], v168 offset:0\n\t" \
    "ds_read_b64_tr_b16 v[98:99], v168 offset:1024\n\t" \
    "v_lshlrev_b32 v52, 16, v128\n\t" \
    "v_and_b32 v53, v56, v128\n\t" \
    "v_lshlrev_b32 v54, 16, v144\n\t" \
    "v_and_b32 v55, v56, v144\n\t" \
    "v_mfma_f32_32x32x16_bf16 v[16:31], v[144:147], v[80:83], v[16:31]\n\t" \
    "ds_read_b64_tr_b16 v[112:113], v172 offset:0\n\t" \
    "ds_read_b64_tr_b16 v[114:115], v172 offset:1024\n\t" \
    "ds_read_b128 v[68:71], v161 offset:0\n\t" \
    "v_add_f32 v48, v48, v52\n\t" \
    "v_add_f32 v49, v49, v53\n\t" \
    "v_add_f32 v50, v50, v54\n\t" \
    "v_add_f32 v51, v51, v55\n\t" \
    "v_mfma_f32_32x32x16_bf16 v[0:15], v[132:135], v[84:87], v[0:15]\n\t" \
    "ds_read_b64_tr_b16 v[100:101], v169 offset:0\n\t" \
    "ds_read_b64_tr_b16 v[102:103], v169 offset:1024\n\t" \
    "ds_read_b64_tr_b16 v[116:117], v173 offset:0\n\t" \
    "v_lshlrev_b32 v52, 16, v129\n\t" \
    "v_and_b32 v53, v56, v129\n\t" \
    "v_lshlrev_b32 v54, 16, v145\n\t" \
    "v_and_b32 v55, v56, v145\n\t" \
    "v_mfma_f32_32x32x16_bf16 v[16:31], v[148:151], v[84:87], v[16:31]\n\t" \
    "ds_read_b64_tr_b16 v[118:119], v173 offset:1024\n\t" \
    "ds_read_b128 v[72:75], v162 offset:0\n\t" \
    "ds_read_b64_tr_b16 v[104:105], v170 offset:0\n\t" \
    "v_add_f32 v48, v48, v52\n\t" \
    "v_add_f32 v49, v49, v53\n\t" \
    "v_add_f32 v50, v50, v54\n\t" \
    "v_add_f32 v51, v51, v55\n\t" \
    "v_mfma_f32_32x32x16_bf16 v[0:15], v[136:139], v[88:91], v[0:15]\n\t" \
    "ds_read_b64_tr_b16 v[106:107], v170 offset:1024\n\t" \
    "ds_read_b64_tr_b16 v[120:121], v174 offset:0\n\t" \
    "ds_read_b64_tr_b16 v[122:123], v174 offset:1024\n\t" \
    "v_lshlrev_b32 v52, 16, v130\n\t" \
    "v_and_b32 v53, v56, v130\n\t" \
    "v_lshlrev_b32 v54, 16, v146\n\t" \
    "v_and_b32 v55, v56, v146\n\t" \
    "v_mfma_f32_32x32x16_bf16 v[16:31], v[152:155], v[88:91], v[16:31]\n\t" \
    "ds_read_b128 v[76:79], v163 offset:0\n\t" \
    "ds_read_b64_tr_b16 v[108:109], v171 offset:0\n\t" \
    "ds_read_b64_tr_b16 v[110:111], v171 offset:1024\n\t" \
    "v_add_f32 v48, v48, v52\n\t" \
    "v_add_f32 v49, v49, v53\n\t" \
    "v_add_f32 v50, v50, v54\n\t" \
    "v_add_f32 v51, v51, v55\n\t" \
    "v_mfma_f32_32x32x16_bf16 v[0:15], v[140:143], v[92:95], v[0:15]\n\t" \
    "ds_read_b64_tr_b16 v[124:125], v175 offset:0\n\t" \
    "ds_read_b64_tr_b16 v[126:127], v175 offset:1024\n\t" \
    "v_lshlrev_b32 v52, 16, v131\n\t" \
    "v_and_b32 v53, v56, v131\n\t" \
    "v_lshlrev_b32 v54, 16, v147\n\t" \
    "v_and_b32 v55, v56, v147\n\t" \
    "v_mfma_f32_32x32x16_bf16 v[16:31], v[156:159], v[92:95], v[16:31]\n\t" \
    "v_add_f32 v48, v48, v52\n\t" \
    "v_add_f32 v49, v49, v53\n\t" \
    "v_add_f32 v50, v50, v54\n\t" \
    "v_add_f32 v51, v51, v55\n\t" \
    "v_mfma_f32_4x4x4_16b_bf16 v[32:35], v[80:81], v[80:81], v[32:35]\n\t" \
    "v_mfma_f32_4x4x4_16b_bf16 v[58:61], v[80:81], v[62:63], v[58:61]\n\t" \
    "v_mfma_f32_4x4x4_16b_bf16 v[36:39], v[82:83], v[82:83], v[36:39]\n\t" \
    "v_mfma_f32_4x4x4_16b_bf16 v[190:193], v[82:83], v[62:63], v[190:193]\n\t" \
    "v_mfma_f32_4x4x4_16b_bf16 v[40:43], v[84:85], v[84:85], v[40:43]\n\t" \
    "v_mfma_f32_4x4x4_16b_bf16 v[58:61], v[84:85], v[62:63], v[58:61]\n\t" \
    "v_mfma_f32_4x4x4_16b_bf16 v[44:47], v[86:87], v[86:87], v[44:47]\n\t" \
    "v_mfma_f32_4x4x4_16b_bf16 v[190:193], v[86:87], v[62:63], v[190:193]\n\t" \
    "s_waitcnt vmcnt(0) lgkmcnt(0)\n\t" \
    "s_barrier\n\t" \
    "v_mfma_f32_32x32x16_bf16 v[0:15], v[96:99], v[64:67], v[0:15]\n\t" \
    "ds_read_b128 v[80:83], v160 offset:32768\n\t" \
    "ds_read_b64_tr_b16 v[128:129], v168 offset:32768\n\t" \
    "ds_read_b64_tr_b16 v[130:131], v168 offset:33792\n\t" \
    "v_lshlrev_b32 v52, 16, v96\n\t" \
    "v_and_b32 v53, v56, v96\n\t" \
    "v_lshlrev_b32 v54, 16, v112\n\t" \
    "v_and_b32 v55, v56, v112\n\t" \
    "v_mfma_f32_32x32x16_bf16 v[16:31], v[112:115], v[64:67], v[16:31]\n\t" \
    "ds_read_b64_tr_b16 v[144:145], v172 offset:32768\n\t" \
    "ds_read_b64_tr_b16 v[146:147], v172 offset:33792\n\t" \
    "ds_read_b128 v[84:87], v161 offset:32768\n\t" \
    "v_add_f32 v48, v48, v52\n\t" \
    "v_add_f32 v49, v49, v53\n\t" \
    "v_add_f32 v50, v50, v54\n\t" \
    "v_add_f32 v51, v51, v55\n\t" \
    "v_mfma_f32_32x32x16_bf16 v[0:15], v[100:103], v[68:71], v[0:15]\n\t" \
    "ds_read_b64_tr_b16 v[132:133], v169 offset:32768\n\t" \
    "ds_read_b64_tr_b16 v[134:135], v169 offset:33792\n\t" \
    "ds_read_b64_tr_b16 v[148:149], v173 offset:32768\n\t" \
    "v_lshlrev_b32 v52, 16, v97\n\t" \
    "v_and_b32 v53, v56, v97\n\t" \
    "v_lshlrev_b32 v54, 16, v113\n\t" \
    "v_and_b32 v55, v56, v113\n\t" \
    "v_mfma_f32_32x32x16_bf16 v[16:31], v[116:119], v[68:71], v[16:31]\n\t" \
    "ds_read_b64_tr_b16 v[150:151], v173 offset:33792\n\t" \
    "ds_read_b128 v[88:91], v162 offset:32768\n\t" \
    "ds_read_b64_tr_b16 v[136:137], v170 offset:32768\n\t" \
    "v_add_f32 v48, v48, v52\n\t" \
    "v_add_f32 v49, v49, v53\n\t" \
    "v_add_f32 v50, v50, v54\n\t" \
    "v_add_f32 v51, v51, v55\n\t" \
    "v_mfma_f32_32x32x16_bf16 v[0:15], v[104:107], v[72:75], v[0:15]\n\t" \
    "ds_read_b64_tr_b16 v[138:139], v170 offset:33792\n\t" \
    "ds_read_b64_tr_b16 v[152:153], v174 offset:32768\n\t" \
    "ds_read_b64_tr_b16 v[154:155], v174 offset:33792\n\t" \
    "v_lshlrev_b32 v52, 16, v98\n\t" \
    "v_and_b32 v53, v56, v98\n\t" \
    "v_lshlrev_b32 v54, 16, v114\n\t" \
    "v_and_b32 v55, v56, v114\n\t" \
    "v_mfma_f32_32x32x16_bf16 v[16:31], v[120:123], v[72:75], v[16:31]\n\t" \
    "ds_read_b128 v[92:95], v163 offset:32768\n\t" \
    "ds_read_b64_tr_b16 v[140:141], v171 offset:32768\n\t" \
    "ds_read_b64_tr_b16 v[142:143], v171 offset:33792\n\t" \
    "v_add_f32 v48, v48, v52\n\t" \
    "v_add_f32 v49, v49, v53\n\t" \
    "v_add_f32 v50, v50, v54\n\t" \
    "v_add_f32 v51, v51, v55\n\t" \
    "v_mfma_f32_32x32x16_bf16 v[0:15], v[108:111], v[76:79], v[0:15]\n\t" \
    "ds_read_b64_tr_b16 v[156:157], v175 offset:32768\n\t" \
    "ds_read_b64_tr_b16 v[158:159], v175 offset:33792\n\t" \
    "v_lshlrev_b32 v52, 16, v99\n\t" \
    "v_and_b32 v53, v56, v99\n\t" \
    "v_lshlrev_b32 v54, 16, v115\n\t" \
    "v_and_b32 v55, v56, v115\n\t" \
    "v_mfma_f32_32x32x16_bf16 v[16:31], v[124:127], v[76:79], v[16:31]\n\t" \
    "v_add_f32 v48, v48, v52\n\t" \
    "v_add_f32 v49, v49, v53\n\t" \
    "v_add_f32 v50, v50, v54\n\t" \
    "v_add_f32 v51, v51, v55\n\t" \
    "v_mfma_f32_4x4x4_16b_bf16 v[32:35], v[64:65], v[64:65], v[32:35]\n\t" \
    "v_mfma_f32_4x4x4_16b_bf16 v[58:61], v[64:65], v[62:63], v[58:61]\n\t" \
    "v_mfma_f32_4x4x4_16b_bf16 v[36:39], v[66:67], v[66:67], v[36:39]\n\t" \
    "v_mfma_f32_4x4x4_16b_bf16 v[190:193], v[66:67], v[62:63], v[190:193]\n\t" \
    "v_mfma_f32_4x4x4_16b_bf16 v[40:43], v[68:69], v[68:69], v[40:43]\n\t" \
    "v_mfma_f32_4x4x4_16b_bf16 v[58:61], v[68:69], v[62:63], v[58:61]\n\t" \
    "v_mfma_f32_4x4x4_16b_bf16 v[44:47], v[70:71], v[70:71], v[44:47]\n\t" \
    "v_mfma_f32_4x4x4_16b_bf16 v[190:193], v[70:71], v[62:63], v[190:193]\n\t" \
    "s_waitcnt lgkmcnt(0)\n\t" \
    "v_mfma_f32_32x32x16_bf16 v[0:15], v[128:131], v[80:83], v[0:15]\n\t" \
    "v_lshlrev_b32 v52, 16, v128\n\t" \
    "v_and_b32 v53, v56, v128\n\t" \
    "v_lshlrev_b32 v54, 16, v144\n\t" \
    "v_and_b32 v55, v56, v144\n\t" \
    "v_mfma_f32_32x32x16_bf16 v[16:31], v[144:147], v[80:83], v[16:31]\n\t" \
    "v_add_f32 v48, v48, v52\n\t" \
    "v_add_f32 v49, v49, v53\n\t" \
    "v_add_f32 v50, v50, v54\n\t" \
    "v_add_f32 v51, v51, v55\n\t" \
    "v_mfma_f32_32x32x16_bf16 v[0:15], v[132:135], v[84:87], v[0:15]\n\t" \
    "v_lshlrev_b32 v52, 16, v129\n\t" \
    "v_and_b32 v53, v56, v129\n\t" \
    "v_lshlrev_b32 v54, 16, v145\n\t" \
    "v_and_b32 v55, v56, v145\n\t" \
    "v_mfma_f32_32x32x16_bf16 v[16:31], v[148:151], v[84:87], v[16:31]\n\t" \
    "v_add_f32 v48, v48, v52\n\t" \
    "v_add_f32 v49, v49, v53\n\t" \
    "v_add_f32 v50, v50, v54\n\t" \
    "v_add_f32 v51, v51, v55\n\t" \
    "v_mfma_f32_32x32x16_bf16 v[0:15], v[136:139], v[88:91], v[0:15]\n\t" \
    "v_lshlrev_b32 v52, 16, v130\n\t" \
    "v_and_b32 v53, v56, v130\n\t" \
    "v_lshlrev_b32 v54, 16, v146\n\t" \
    "v_and_b32 v55, v56, v146\n\t" \
    "v_mfma_f32_32x32x16_bf16 v[16:31], v[152:155], v[88:91], v[16:31]\n\t" \
    "v_add_f32 v48, v48, v52\n\t" \
    "v_add_f32 v49, v49, v53\n\t" \
    "v_add_f32 v50, v50, v54\n\t" \
    "v_add_f32 v51, v51, v55\n\t" \
    "v_mfma_f32_32x32x16_bf16 v[0:15], v[140:143], v[92:95], v[0:15]\n\t" \
    "v_lshlrev_b32 v52, 16, v131\n\t" \
    "v_and_b32 v53, v56, v131\n\t" \
    "v_lshlrev_b32 v54, 16, v147\n\t" \
    "v_and_b32 v55, v56, v147\n\t" \
    "v_mfma_f32_32x32x16_bf16 v[16:31], v[156:159], v[92:95], v[16:31]\n\t" \
    "v_add_f32 v48, v48, v52\n\t" \
    "v_add_f32 v49, v49, v53\n\t" \
    "v_add_f32 v50, v50, v54\n\t" \
    "v_add_f32 v51, v51, v55\n\t" \
    "v_mfma_f32_4x4x4_16b_bf16 v[32:35], v[80:81], v[80:81], v[32:35]\n\t" \
    "v_mfma_f32_4x4x4_16b_bf16 v[58:61], v[80:81], v[62:63], v[58:61]\n\t" \
    "v_mfma_f32_4x4x4_16b_bf16 v[36:39], v[82:83], v[82:83], v[36:39]\n\t" \
    "v_mfma_f32_4x4x4_16b_bf16 v[190:193], v[82:83], v[62:63], v[190:193]\n\t" \
    "v_mfma_f32_4x4x4_16b_bf16 v[40:43], v[84:85], v[84:85], v[40:43]\n\t" \
    "v_mfma_f32_4x4x4_16b_bf16 v[58:61], v[84:85], v[62:63], v[58:61]\n\t" \
    "v_mfma_f32_4x4x4_16b_bf16 v[44:47], v[86:87], v[86:87], v[44:47]\n\t" \
    "v_mfma_f32_4x4x4_16b_bf16 v[190:193], v[86:87], v[62:63], v[190:193]\n\t" \
    "s_nop 15\n\t" \
    "s_mov_b32 m0, %[sv]\n\t"
#define QKV_CLOB "v52","v53","v54","v55","v56","v57","v62","v63","v64","v65","v66","v67","v68","v69","v70","v71","v72","v73","v74","v75","v76","v77","v78","v79","v80","v81","v82","v83","v84","v85","v86","v87","v88","v89","v90","v91","v92","v93","v94","v95","v96","v97","v98","v99","v100","v101","v102","v103","v104","v105","v106","v107","v108","v109","v110","v111","v112","v113","v114","v115","v116","v117","v118","v119","v120","v121","v122","v123","v124","v125","v126","v127","v128","v129","v130","v131","v132","v133","v134","v135","v136","v137","v138","v139","v140","v141","v142","v143","v144","v145","v146","v147","v148","v149","v150","v151","v152","v153","v154","v155","v156","v157","v158","v159"
#define D_DMA0_ASM \
    "s_mov_b32 %[sv], m0\n\t" \
    "s_mul_i32 %[st], %[rstep], 0\n\t" \
    "s_add_u32 m0, %[ml2], 0\n\t" \
    "s_nop 0\n\t" \
    "buffer_load_dwordx4 v208, %[rs2], %[st] offen offset:0 lds\n\t" \
    "buffer_load_dwordx4 v209, %[rs2], %[st] offen offset:1024 lds\n\t" \
    "buffer_load_dwordx4 v210, %[rs2], %[st] offen offset:2048 lds\n\t" \
    "buffer_load_dwordx4 v211, %[rs2], %[st] offen offset:3072 lds\n\t" \
    "s_mul_i32 %[st], %[rstep], 1\n\t" \
    "s_add_u32 m0, %[ml2], 4096\n\t" \
    "s_nop 0\n\t" \
    "buffer_load_dwordx4 v208, %[rs2], %[st] offen offset:0 lds\n\t" \
    "buffer_load_dwordx4 v209, %[rs2], %[st] offen offset:1024 lds\n\t" \
    "buffer_load_dwordx4 v210, %[rs2], %[st] offen offset:2048 lds\n\t" \
    "buffer_load_dwordx4 v211, %[rs2], %[st] offen offset:3072 lds\n\t" \
    "s_mul_i32 %[st], %[rstep], 2\n\t" \
    "s_add_u32 m0, %[ml2], 8192\n\t" \
    "s_nop 0\n\t" \
    "buffer_load_dwordx4 v208, %[rs2], %[st] offen offset:0 lds\n\t" \
    "buffer_load_dwordx4 v209, %[rs2], %[st] offen offset:1024 lds\n\t" \
    "buffer_load_dwordx4 v210, %[rs2], %[st] offen offset:2048 lds\n\t" \
    "buffer_load_dwordx4 v211, %[rs2], %[st] offen offset:3072 lds\n\t" \
    "s_mul_i32 %[st], %[rstep], 3\n\t" \
    "s_add_u32 m0, %[ml2], 12288\n\t" \
    "s_nop 0\n\t" \
    "buffer_load_dwordx4 v208, %[rs2], %[st] offen offset:0 lds\n\t" \
    "buffer_load_dwordx4 v209, %[rs2], %[st] offen offset:1024 lds\n\t" \
    "buffer_load_dwordx4 v210, %[rs2], %[st] offen offset:2048 lds\n\t" \
    "buffer_load_dwordx4 v211, %[rs2], %[st] offen offset:3072 lds\n\t" \
    "s_mul_i32 %[st], %[rstep], 0\n\t" \
    "s_add_u32 m0, %[ml], 0\n\t" \
    "s_nop 0\n\t" \
    "buffer_load_dwordx4 v208, %[rs], %[st] offen offset:0 lds\n\t" \
    "buffer_load_dwordx4 v209, %[rs], %[st] offen offset:1024 lds\n\t" \
    "buffer_load_dwordx4 v210, %[rs], %[st] offen offset:2048 lds\n\t" \
    "buffer_load_dwordx4 v211, %[rs], %[st] offen offset:3072 lds\n\t" \
    "s_mul_i32 %[st], %[rstep], 1\n\t" \
    "s_add_u32 m0, %[ml], 4096\n\t" \
    "s_nop 0\n\t" \
    "buffer_load_dwordx4 v208, %[rs], %[st] offen offset:0 lds\n\t" \
    "buffer_load_dwordx4 v209, %[rs], %[st] offen offset:1024 lds\n\t" \
    "buffer_load_dwordx4 v210, %[rs], %[st] offen offset:2048 lds\n\t" \
    "buffer_load_dwordx4 v211, %[rs], %[st] offen offset:3072 lds\n\t" \
    "s_mul_i32 %[st], %[rstep], 2\n\t" \
    "s_add_u32 m0, %[ml], 8192\n\t" \
    "s_nop 0\n\t" \
    "buffer_load_dwordx4 v208, %[rs], %[st] offen offset:0 lds\n\t" \
    "buffer_load_dwordx4 v209, %[rs], %[st] offen offset:1024 lds\n\t" \
    "buffer_load_dwordx4 v210, %[rs], %[st] offen offset:2048 lds\n\t" \
    "buffer_load_dwordx4 v211, %[rs], %[st] offen offset:3072 lds\n\t" \
    "s_mul_i32 %[st], %[rstep], 3\n\t" \
    "s_add_u32 m0, %[ml], 12288\n\t" \
    "s_nop 0\n\t" \
    "buffer_load_dwordx4 v208, %[rs], %[st] offen offset:0 lds\n\t" \
    "buffer_load_dwordx4 v209, %[rs], %[st] offen offset:1024 lds\n\t" \
    "buffer_load_dwordx4 v210, %[rs], %[st] offen offset:2048 lds\n\t" \
    "buffer_load_dwordx4 v211, %[rs], %[st] offen offset:3072 lds\n\t" \
    "s_mov_b32 m0, %[sv]\n\t"
#define D_PRE_ASM \
    "s_waitcnt vmcnt(0)\n\t" \
    "ds_read_b64_tr_b16 v[64:65], v212 offset:0\n\t" \
    "ds_read_b64_tr_b16 v[66:67], v212 offset:512\n\t" \
    "ds_read_b64_tr_b16 v[68:69], v213 offset:0\n\t" \
    "ds_read_b64_tr_b16 v[70:71], v213 offset:512\n\t" \
    "ds_read_b64_tr_b16 v[72:73], v212 offset:2048\n\t" \
    "ds_read_b64_tr_b16 v[74:75], v212 offset:2560\n\t" \
    "ds_read_b64_tr_b16 v[76:77], v213 offset:2048\n\t" \
    "ds_read_b64_tr_b16 v[78:79], v213 offset:2560\n\t" \
    "ds_read_b64_tr_b16 v[80:81], v212 offset:4096\n\t" \
    "ds_read_b64_tr_b16 v[82:83], v212 offset:4608\n\t" \
    "ds_read_b64_tr_b16 v[84:85], v213 offset:4096\n\t" \
    "ds_read_b64_tr_b16 v[86:87], v213 offset:4608\n\t" \
    "ds_read_b64_tr_b16 v[88:89], v212 offset:6144\n\t" \
    "ds_read_b64_tr_b16 v[90:91], v212 offset:6656\n\t" \
    "ds_read_b64_tr_b16 v[92:93], v213 offset:6144\n\t" \
    "ds_read_b64_tr_b16 v[94:95], v213 offset:6656\n\t" \
    "ds_read_b64_tr_b16 v[96:97], v212 offset:8192\n\t" \
    "ds_read_b64_tr_b16 v[98:99], v212 offset:8704\n\t" \
    "ds_read_b64_tr_b16 v[100:101], v213 offset:8192\n\t" \
    "ds_read_b64_tr_b16 v[102:103], v213 offset:8704\n\t" \
    "ds_read_b64_tr_b16 v[104:105], v212 offset:10240\n\t" \
    "ds_read_b64_tr_b16 v[106:107], v212 offset:10752\n\t" \
    "ds_read_b64_tr_b16 v[108:109], v213 offset:10240\n\t" \
    "ds_read_b64_tr_b16 v[110:111], v213 offset:10752\n\t" \
    "ds_read_b64_tr_b16 v[112:113], v212 offset:12288\n\t" \
    "ds_read_b64_tr_b16 v[114:115], v212 offset:12800\n\t" \
    "ds_read_b64_tr_b16 v[116:117], v213 offset:12288\n\t" \
    "ds_read_b64_tr_b16 v[118:119], v213 offset:12800\n\t" \
    "ds_read_b64_tr_b16 v[120:121], v212 offset:14336\n\t" \
    "ds_read_b64_tr_b16 v[122:123], v212 offset:14848\n\t" \
    "ds_read_b64_tr_b16 v[124:125], v213 offset:14336\n\t" \
    "ds_read_b64_tr_b16 v[126:127], v213 offset:14848\n\t" \
    "s_waitcnt lgkmcnt(0)\n\t"
#define D_DMA1_ASM \
    "s_mov_b32 %[sv], m0\n\t" \
    "s_mul_i32 %[st], %[rstep], 4\n\t" \
    "s_add_u32 m0, %[ml], 0\n\t" \
    "s_nop 0\n\t" \
    "buffer_load_dwordx4 v208, %[rs], %[st] offen offset:0 lds\n\t" \
    "buffer_load_dwordx4 v209, %[rs], %[st] offen offset:1024 lds\n\t" \
    "buffer_load_dwordx4 v210, %[rs], %[st] offen offset:2048 lds\n\t" \
    "buffer_load_dwordx4 v211, %[rs], %[st] offen offset:3072 lds\n\t" \
    "s_mul_i32 %[st], %[rstep], 5\n\t" \
    "s_add_u32 m0, %[ml], 4096\n\t" \
    "s_nop 0\n\t" \
    "buffer_load_dwordx4 v208, %[rs], %[st] offen offset:0 lds\n\t" \
    "buffer_load_dwordx4 v209, %[rs], %[st] offen offset:1024 lds\n\t" \
    "buffer_load_dwordx4 v210, %[rs], %[st] offen offset:2048 lds\n\t" \
    "buffer_load_dwordx4 v211, %[rs], %[st] offen offset:3072 lds\n\t" \
    "s_mul_i32 %[st], %[rstep], 6\n\t" \
    "s_add_u32 m0, %[ml], 8192\n\t" \
    "s_nop 0\n\t" \
    "buffer_load_dwordx4 v208, %[rs], %[st] offen offset:0 lds\n\t" \
    "buffer_load_dwordx4 v209, %[rs], %[st] offen offset:1024 lds\n\t" \
    "buffer_load_dwordx4 v210, %[rs], %[st] offen offset:2048 lds\n\t" \
    "buffer_load_dwordx4 v211, %[rs], %[st] offen offset:3072 lds\n\t" \
    "s_mul_i32 %[st], %[rstep], 7\n\t" \
    "s_add_u32 m0, %[ml], 12288\n\t" \
    "s_nop 0\n\t" \
    "buffer_load_dwordx4 v208, %[rs], %[st] offen offset:0 lds\n\t" \
    "buffer_load_dwordx4 v209, %[rs], %[st] offen offset:1024 lds\n\t" \
    "buffer_load_dwordx4 v210, %[rs], %[st] offen offset:2048 lds\n\t" \
    "buffer_load_dwordx4 v211, %[rs], %[st] offen offset:3072 lds\n\t" \
    "s_mov_b32 m0, %[sv]\n\t"
#define D_MAIN_ASM \
    "s_mov_b32 %[st], 0\n\t" \
    "buffer_load_dwordx4 v[128:131], v214, %[rh], %[st] offen offset:0 sc1\n\t" \
    "buffer_load_dwordx4 v[132:135], v214, %[rh], %[st] offen offset:1024 sc1\n\t" \
    "s_mov_b32 %[st], 20480\n\t" \
    "buffer_load_dwordx4 v[136:139], v214, %[rh], %[st] offen offset:0 sc1\n\t" \
    "buffer_load_dwordx4 v[140:143], v214, %[rh], %[st] offen offset:1024 sc1\n\t" \
    "s_mov_b32 %[st], 40960\n\t" \
    "buffer_load_dwordx4 v[144:147], v214, %[rh], %[st] offen offset:0 sc1\n\t" \
    "buffer_load_dwordx4 v[148:151], v214, %[rh], %[st] offen offset:1024 sc1\n\t" \
    "s_mov_b32 %[st], 61440\n\t" \
    "buffer_load_dwordx4 v[152:155], v214, %[rh], %[st] offen offset:0 sc1\n\t" \
    "buffer_load_dwordx4 v[156:159], v214, %[rh], %[st] offen offset:1024 sc1\n\t" \
    "s_mov_b32 %[st], 81920\n\t" \
    "buffer_load_dwordx4 v[160:163], v214, %[rh], %[st] offen offset:0 sc1\n\t" \
    "buffer_load_dwordx4 v[164:167], v214, %[rh], %[st] offen offset:1024 sc1\n\t" \
    "s_mov_b32 %[st], 102400\n\t" \
    "buffer_load_dwordx4 v[168:171], v214, %[rh], %[st] offen offset:0 sc1\n\t" \
    "buffer_load_dwordx4 v[172:175], v214, %[rh], %[st] offen offset:1024 sc1\n\t" \
    "s_mov_b32 %[st], 122880\n\t" \
    "buffer_load_dwordx4 v[176:179], v214, %[rh], %[st] offen offset:0 sc1\n\t" \
    "buffer_load_dwordx4 v[180:183], v214, %[rh], %[st] offen offset:1024 sc1\n\t" \
    "s_mov_b32 %[st], 143360\n\t" \
    "buffer_load_dwordx4 v[184:187], v214, %[rh], %[st] offen offset:0 sc1\n\t" \
    "buffer_load_dwordx4 v[188:191], v214, %[rh], %[st] offen offset:1024 sc1\n\t" \
    "v_mov_b32 v0, 0\n\t" \
    "v_mov_b32 v1, 0\n\t" \
    "v_mov_b32 v2, 0\n\t" \
    "v_mov_b32 v3, 0\n\t" \
    "v_mov_b32 v4, 0\n\t" \
    "v_mov_b32 v5, 0\n\t" \
    "v_mov_b32 v6, 0\n\t" \
    "v_mov_b32 v7, 0\n\t" \
    "v_mov_b32 v8, 0\n\t" \
    "v_mov_b32 v9, 0\n\t" \
    "v_mov_b32 v10, 0\n\t" \
    "v_mov_b32 v11, 0\n\t" \
    "v_mov_b32 v12, 0\n\t" \
    "v_mov_b32 v13, 0\n\t" \
    "v_mov_b32 v14, 0\n\t" \
    "v_mov_b32 v15, 0\n\t" \
    "v_mov_b32 v16, 0\n\t" \
    "v_mov_b32 v17, 0\n\t" \
    "v_mov_b32 v18, 0\n\t" \
    "v_mov_b32 v19, 0\n\t" \
    "v_mov_b32 v20, 0\n\t" \
    "v_mov_b32 v21, 0\n\t" \
    "v_mov_b32 v22, 0\n\t" \
    "v_mov_b32 v23, 0\n\t" \
    "v_mov_b32 v24, 0\n\t" \
    "v_mov_b32 v25, 0\n\t" \
    "v_mov_b32 v26, 0\n\t" \
    "v_mov_b32 v27, 0\n\t" \
    "v_mov_b32 v28, 0\n\t" \
    "v_mov_b32 v29, 0\n\t" \
    "v_mov_b32 v30, 0\n\t" \
    "v_mov_b32 v31, 0\n\t" \
    "v_mov_b32 v32, 0\n\t" \
    "v_mov_b32 v33, 0\n\t" \
    "v_mov_b32 v34, 0\n\t" \
    "v_mov_b32 v35, 0\n\t" \
    "v_mov_b32 v36, 0\n\t" \
    "v_mov_b32 v37, 0\n\t" \
    "v_mov_b32 v38, 0\n\t" \
    "v_mov_b32 v39, 0\n\t" \
    "v_mov_b32 v40, 0\n\t" \
    "v_mov_b32 v41, 0\n\t" \
    "v_mov_b32 v42, 0\n\t" \
    "v_mov_b32 v43, 0\n\t" \
    "v_mov_b32 v44, 0\n\t" \
    "v_mov_b32 v45, 0\n\t" \
    "v_mov_b32 v46, 0\n\t" \
    "v_mov_b32 v47, 0\n\t" \
    "v_mov_b32 v48, 0\n\t" \
    "v_mov_b32 v49, 0\n\t" \
    "v_mov_b32 v50, 0\n\t" \
    "v_mov_b32 v51, 0\n\t" \
    "v_mov_b32 v52, 0\n\t" \
    "v_mov_b32 v53, 0\n\t" \
    "v_mov_b32 v54, 0\n\t" \
    "v_mov_b32 v55, 0\n\t" \
    "v_mov_b32 v56, 0\n\t" \
    "v_mov_b32 v57, 0\n\t" \
    "v_mov_b32 v58, 0\n\t" \
    "v_mov_b32 v59, 0\n\t" \
    "v_mov_b32 v60, 0\n\t" \
    "v_mov_b32 v61, 0\n\t" \
    "v_mov_b32 v62, 0\n\t" \
    "v_mov_b32 v63, 0\n\t" \
    "s_waitcnt vmcnt(14)\n\t" \
    "v_mfma_f32_32x32x16_bf16 v[0:15], v[64:67], v[128:131], v[0:15]\n\t" \
    "v_mfma_f32_32x32x16_bf16 v[32:47], v[68:71], v[128:131], v[32:47]\n\t" \
    "v_mfma_f32_32x32x16_bf16 v[16:31], v[64:67], v[132:135], v[16:31]\n\t" \
    "v_mfma_f32_32x32x16_bf16 v[48:63], v[68:71], v[132:135], v[48:63]\n\t" \
    "s_mov_b32 %[st], 163840\n\t" \
    "buffer_load_dwordx4 v[128:131], v214, %[rh], %[st] offen offset:0 sc1\n\t" \
    "buffer_load_dwordx4 v[132:135], v214, %[rh], %[st] offen offset:1024 sc1\n\t" \
    "s_waitcnt vmcnt(14)\n\t" \
    "v_mfma_f32_32x32x16_bf16 v[0:15], v[72:75], v[136:139], v[0:15]\n\t" \
    "v_mfma_f32_32x32x16_bf16 v[32:47], v[76:79], v[136:139], v[32:47]\n\t" \
    "v_mfma_f32_32x32x16_bf16 v[16:31], v[72:75], v[140:143], v[16:31]\n\t" \
    "v_mfma_f32_32x32x16_bf16 v[48:63], v[76:79], v[140:143], v[48:63]\n\t" \
    "s_mov_b32 %[st], 184320\n\t" \
    "buffer_load_dwordx4 v[136:139], v214, %[rh], %[st] offen offset:0 sc1\n\t" \
    "buffer_load_dwordx4 v[140:143], v214, %[rh], %[st] offen offset:1024 sc1\n\t" \
    "s_waitcnt vmcnt(14)\n\t" \
    "v_mfma_f32_32x32x16_bf16 v[0:15], v[80:83], v[144:147], v[0:15]\n\t" \
    "v_mfma_f32_32x32x16_bf16 v[32:47], v[84:87], v[144:147], v[32:47]\n\t" \
    "v_mfma_f32_32x32x16_bf16 v[16:31], v[80:83], v[148:151], v[16:31]\n\t" \
    "v_mfma_f32_32x32x16_bf16 v[48:63], v[84:87], v[148:151], v[48:63]\n\t" \
    "s_mov_b32 %[st], 204800\n\t" \
    "buffer_load_dwordx4 v[144:147], v214, %[rh], %[st] offen offset:0 sc1\n\t" \
    "buffer_load_dwordx4 v[148:151], v214, %[rh], %[st] offen offset:1024 sc1\n\t" \
    "s_waitcnt vmcnt(14)\n\t" \
    "v_mfma_f32_32x32x16_bf16 v[0:15], v[88:91], v[152:155], v[0:15]\n\t" \
    "v_mfma_f32_32x32x16_bf16 v[32:47], v[92:95], v[152:155], v[32:47]\n\t" \
    "v_mfma_f32_32x32x16_bf16 v[16:31], v[88:91], v[156:159], v[16:31]\n\t" \
    "v_mfma_f32_32x32x16_bf16 v[48:63], v[92:95], v[156:159], v[48:63]\n\t" \
    "s_mov_b32 %[st], 225280\n\t" \
    "buffer_load_dwordx4 v[152:155], v214, %[rh], %[st] offen offset:0 sc1\n\t" \
    "buffer_load_dwordx4 v[156:159], v214, %[rh], %[st] offen offset:1024 sc1\n\t" \
    "s_waitcnt vmcnt(14)\n\t" \
    "v_mfma_f32_32x32x16_bf16 v[0:15], v[96:99], v[160:163], v[0:15]\n\t" \
    "v_mfma_f32_32x32x16_bf16 v[32:47], v[100:103], v[160:163], v[32:47]\n\t" \
    "v_mfma_f32_32x32x16_bf16 v[16:31], v[96:99], v[164:167], v[16:31]\n\t" \
    "v_mfma_f32_32x32x16_bf16 v[48:63], v[100:103], v[164:167], v[48:63]\n\t" \
    "s_mov_b32 %[st], 245760\n\t" \
    "buffer_load_dwordx4 v[160:163], v214, %[rh], %[st] offen offset:0 sc1\n\t" \
    "buffer_load_dwordx4 v[164:167], v214, %[rh], %[st] offen offset:1024 sc1\n\t" \
    "s_waitcnt vmcnt(14)\n\t" \
    "v_mfma_f32_32x32x16_bf16 v[0:15], v[104:107], v[168:171], v[0:15]\n\t" \
    "v_mfma_f32_32x32x16_bf16 v[32:47], v[108:111], v[168:171], v[32:47]\n\t" \
    "v_mfma_f32_32x32x16_bf16 v[16:31], v[104:107], v[172:175], v[16:31]\n\t" \
    "v_mfma_f32_32x32x16_bf16 v[48:63], v[108:111], v[172:175], v[48:63]\n\t" \
    "s_mov_b32 %[st], 266240\n\t" \
    "buffer_load_dwordx4 v[168:171], v214, %[rh], %[st] offen offset:0 sc1\n\t" \
    "buffer_load_dwordx4 v[172:175], v214, %[rh], %[st] offen offset:1024 sc1\n\t" \
    "s_waitcnt vmcnt(14)\n\t" \
    "v_mfma_f32_32x32x16_bf16 v[0:15], v[112:115], v[176:179], v[0:15]\n\t" \
    "v_mfma_f32_32x32x16_bf16 v[32:47], v[116:119], v[176:179], v[32:47]\n\t" \
    "v_mfma_f32_32x32x16_bf16 v[16:31], v[112:115], v[180:183], v[16:31]\n\t" \
    "v_mfma_f32_32x32x16_bf16 v[48:63], v[116:119], v[180:183], v[48:63]\n\t" \
    "s_mov_b32 %[st], 286720\n\t" \
    "buffer_load_dwordx4 v[176:179], v214, %[rh], %[st] offen offset:0 sc1\n\t" \
    "buffer_load_dwordx4 v[180:183], v214, %[rh], %[st] offen offset:1024 sc1\n\t" \
    "s_waitcnt vmcnt(14)\n\t" \
    "v_mfma_f32_32x32x16_bf16 v[0:15], v[120:123], v[184:187], v[0:15]\n\t" \
    "ds_read_b64_tr_b16 v[192:193], v212 offset:0\n\t" \
    "ds_read_b64_tr_b16 v[194:195], v212 offset:512\n\t" \
    "v_mfma_f32_32x32x16_bf16 v[32:47], v[124:127], v[184:187], v[32:47]\n\t" \
    "ds_read_b64_tr_b16 v[196:197], v213 offset:0\n\t" \
    "ds_read_b64_tr_b16 v[198:199], v213 offset:512\n\t" \
    "v_mfma_f32_32x32x16_bf16 v[16:31], v[120:123], v[188:191], v[16:31]\n\t" \
    "v_mfma_f32_32x32x16_bf16 v[48:63], v[124:127], v[188:191], v[48:63]\n\t" \
    "s_mov_b32 %[st], 307200\n\t" \
    "buffer_load_dwordx4 v[184:187], v214, %[rh], %[st] offen offset:0 sc1\n\t" \
    "buffer_load_dwordx4 v[188:191], v214, %[rh], %[st] offen offset:1024 sc1\n\t" \
    "s_waitcnt vmcnt(14) lgkmcnt(0)\n\t" \
    "v_mfma_f32_32x32x16_bf16 v[0:15], v[192:195], v[128:131], v[0:15]\n\t" \
    "ds_read_b64_tr_b16 v[200:201], v212 offset:2048\n\t" \
    "ds_read_b64_tr_b16 v[202:203], v212 offset:2560\n\t" \
    "v_mfma_f32_32x32x16_bf16 v[32:47], v[196:199], v[128:131], v[32:47]\n\t" \
    "ds_read_b64_tr_b16 v[204:205], v213 offset:2048\n\t" \
    "ds_read_b64_tr_b16 v[206:207], v213 offset:2560\n\t" \
    "v_mfma_f32_32x32x16_bf16 v[16:31], v[192:195], v[132:135], v[16:31]\n\t" \
    "v_mfma_f32_32x32x16_bf16 v[48:63], v[196:199], v[132:135], v[48:63]\n\t" \
    "s_waitcnt vmcnt(12) lgkmcnt(0)\n\t" \
    "v_mfma_f32_32x32x16_bf16 v[0:15], v[200:203], v[136:139], v[0:15]\n\t" \
    "ds_read_b64_tr_b16 v[192:193], v212 offset:4096\n\t" \
    "ds_read_b64_tr_b16 v[194:195], v212 offset:4608\n\t" \
    "v_mfma_f32_32x32x16_bf16 v[32:47], v[204:207], v[136:139], v[32:47]\n\t" \
    "ds_read_b64_tr_b16 v[196:197], v213 offset:4096\n\t" \
    "ds_read_b64_tr_b16 v[198:199], v213 offset:4608\n\t" \
    "v_mfma_f32_32x32x16_bf16 v[16:31], v[200:203], v[140:143], v[16:31]\n\t" \
    "v_mfma_f32_32x32x16_bf16 v[48:63], v[204:207], v[140:143], v[48:63]\n\t" \
    "s_waitcnt vmcnt(10) lgkmcnt(0)\n\t" \
    "v_mfma_f32_32x32x16_bf16 v[0:15], v[192:195], v[144:147], v[0:15]\n\t" \
    "ds_read_b64_tr_b16 v[200:201], v212 offset:6144\n\t" \
    "ds_read_b64_tr_b16 v[202:203], v212 offset:6656\n\t" \
    "v_mfma_f32_32x32x16_bf16 v[32:47], v[196:199], v[144:147], v[32:47]\n\t" \
    "ds_read_b64_tr_b16 v[204:205], v213 offset:6144\n\t" \
    "ds_read_b64_tr_b16 v[206:207], v213 offset:6656\n\t" \
    "v_mfma_f32_32x32x16_bf16 v[16:31], v[192:195], v[148:151], v[16:31]\n\t" \
    "v_mfma_f32_32x32x16_bf16 v[48:63], v[196:199], v[148:151], v[48:63]\n\t" \
    "s_waitcnt vmcnt(8) lgkmcnt(0)\n\t" \
    "v_mfma_f32_32x32x16_bf16 v[0:15], v[200:203], v[152:155], v[0:15]\n\t" \
    "ds_read_b64_tr_b16 v[192:193], v212 offset:8192\n\t" \
    "ds_read_b64_tr_b16 v[194:195], v212 offset:8704\n\t" \
    "v_mfma_f32_32x32x16_bf16 v[32:47], v[204:207], v[152:155], v[32:47]\n\t" \
    "ds_read_b64_tr_b16 v[196:197], v213 offset:8192\n\t" \
    "ds_read_b64_tr_b16 v[198:199], v213 offset:8704\n\t" \
    "v_mfma_f32_32x32x16_bf16 v[16:31], v[200:203], v[156:159], v[16:31]\n\t" \
    "v_mfma_f32_32x32x16_bf16 v[48:63], v[204:207], v[156:159], v[48:63]\n\t" \
    "s_waitcnt vmcnt(6) lgkmcnt(0)\n\t" \
    "v_mfma_f32_32x32x16_bf16 v[0:15], v[192:195], v[160:163], v[0:15]\n\t" \
    "ds_read_b64_tr_b16 v[200:201], v212 offset:10240\n\t" \
    "ds_read_b64_tr_b16 v[202:203], v212 offset:10752\n\t" \
    "v_mfma_f32_32x32x16_bf16 v[32:47], v[196:199], v[160:163], v[32:47]\n\t" \
    "ds_read_b64_tr_b16 v[204:205], v213 offset:10240\n\t" \
    "ds_read_b64_tr_b16 v[206:207], v213 offset:10752\n\t" \
    "v_mfma_f32_32x32x16_bf16 v[16:31], v[192:195], v[164:167], v[16:31]\n\t" \
    "v_mfma_f32_32x32x16_bf16 v[48:63], v[196:199], v[164:167], v[48:63]\n\t" \
    "s_waitcnt vmcnt(4) lgkmcnt(0)\n\t" \
    "v_mfma_f32_32x32x16_bf16 v[0:15], v[200:203], v[168:171], v[0:15]\n\t" \
    "ds_read_b64_tr_b16 v[192:193], v212 offset:12288\n\t" \
    "ds_read_b64_tr_b16 v[194:195], v212 offset:12800\n\t" \
    "v_mfma_f32_32x32x16_bf16 v[32:47], v[204:207], v[168:171], v[32:47]\n\t" \
    "ds_read_b64_tr_b16 v[196:197], v213 offset:12288\n\t" \
    "ds_read_b64_tr_b16 v[198:199], v213 offset:12800\n\t" \
    "v_mfma_f32_32x32x16_bf16 v[16:31], v[200:203], v[172:175], v[16:31]\n\t" \
    "v_mfma_f32_32x32x16_bf16 v[48:63], v[204:207], v[172:175], v[48:63]\n\t" \
    "s_waitcnt vmcnt(2) lgkmcnt(0)\n\t" \
    "v_mfma_f32_32x32x16_bf16 v[0:15], v[192:195], v[176:179], v[0:15]\n\t" \
    "ds_read_b64_tr_b16 v[200:201], v212 offset:14336\n\t" \
    "ds_read_b64_tr_b16 v[202:203], v212 offset:14848\n\t" \
    "v_mfma_f32_32x32x16_bf16 v[32:47], v[196:199], v[176:179], v[32:47]\n\t" \
    "ds_read_b64_tr_b16 v[204:205], v213 offset:14336\n\t" \
    "ds_read_b64_tr_b16 v[206:207], v213 offset:14848\n\t" \
    "v_mfma_f32_32x32x16_bf16 v[16:31], v[192:195], v[180:183], v[16:31]\n\t" \
    "v_mfma_f32_32x32x16_bf16 v[48:63], v[196:199], v[180:183], v[48:63]\n\t" \
    "s_waitcnt vmcnt(0) lgkmcnt(0)\n\t" \
    "v_mfma_f32_32x32x16_bf16 v[0:15], v[200:203], v[184:187], v[0:15]\n\t" \
    "v_mfma_f32_32x32x16_bf16 v[32:47], v[204:207], v[184:187], v[32:47]\n\t" \
    "v_mfma_f32_32x32x16_bf16 v[16:31], v[200:203], v[188:191], v[16:31]\n\t" \
    "v_mfma_f32_32x32x16_bf16 v[48:63], v[204:207], v[188:191], v[48:63]\n\t" \
    "s_nop 15\n\t"
#define E_DMA0_ASM \
    "s_mov_b32 %[sv], m0\n\t" \
    "s_mul_i32 %[st], %[rstep], 4\n\t" \
    "s_add_u32 m0, %[ml], 0\n\t" \
    "s_nop 0\n\t" \
    "buffer_load_dwordx4 v224, %[rs], %[st] offen offset:0 lds\n\t" \
    "buffer_load_dwordx4 v225, %[rs], %[st] offen offset:1024 lds\n\t" \
    "buffer_load_dwordx4 v226, %[rs], %[st] offen offset:2048 lds\n\t" \
    "buffer_load_dwordx4 v227, %[rs], %[st] offen offset:3072 lds\n\t" \
    "s_mul_i32 %[st], %[rstep], 5\n\t" \
    "s_add_u32 m0, %[ml], 4096\n\t" \
    "s_nop 0\n\t" \
    "buffer_load_dwordx4 v224, %[rs], %[st] offen offset:0 lds\n\t" \
    "buffer_load_dwordx4 v225, %[rs], %[st] offen offset:1024 lds\n\t" \
    "buffer_load_dwordx4 v226, %[rs], %[st] offen offset:2048 lds\n\t" \
    "buffer_load_dwordx4 v227, %[rs], %[st] offen offset:3072 lds\n\t" \
    "s_mov_b32 m0, %[sv]\n\t"
#define E_PRE_ASM \
    "s_waitcnt vmcnt(0)\n\t" \
    "ds_read_b64_tr_b16 v[160:161], v228 offset:0\n\t" \
    "ds_read_b64_tr_b16 v[162:163], v228 offset:1024\n\t" \
    "ds_read_b64_tr_b16 v[164:165], v229 offset:0\n\t" \
    "ds_read_b64_tr_b16 v[166:167], v229 offset:1024\n\t" \
    "ds_read_b64_tr_b16 v[168:169], v230 offset:0\n\t" \
    "ds_read_b64_tr_b16 v[170:171], v230 offset:1024\n\t" \
    "ds_read_b64_tr_b16 v[172:173], v231 offset:0\n\t" \
    "ds_read_b64_tr_b16 v[174:175], v231 offset:1024\n\t" \
    "ds_read_b64_tr_b16 v[176:177], v228 offset:4096\n\t" \
    "ds_read_b64_tr_b16 v[178:179], v228 offset:5120\n\t" \
    "ds_read_b64_tr_b16 v[180:181], v229 offset:4096\n\t" \
    "ds_read_b64_tr_b16 v[182:183], v229 offset:5120\n\t" \
    "ds_read_b64_tr_b16 v[184:185], v230 offset:4096\n\t" \
    "ds_read_b64_tr_b16 v[186:187], v230 offset:5120\n\t" \
    "ds_read_b64_tr_b16 v[188:189], v231 offset:4096\n\t" \
    "ds_read_b64_tr_b16 v[190:191], v231 offset:5120\n\t" \
    "s_waitcnt lgkmcnt(0)\n\t"
#define E_DMA1_ASM \
    "s_mov_b32 %[sv], m0\n\t" \
    "s_mul_i32 %[st], %[rstep], 0\n\t" \
    "s_add_u32 m0, %[ml], 0\n\t" \
    "s_nop 0\n\t" \
    "buffer_load_dwordx4 v224, %[rs], %[st] offen offset:0 lds\n\t" \
    "buffer_load_dwordx4 v225, %[rs], %[st] offen offset:1024 lds\n\t" \
    "buffer_load_dwordx4 v226, %[rs], %[st] offen offset:2048 lds\n\t" \
    "buffer_load_dwordx4 v227, %[rs], %[st] offen offset:3072 lds\n\t" \
    "s_mul_i32 %[st], %[rstep], 1\n\t" \
    "s_add_u32 m0, %[ml], 4096\n\t" \
    "s_nop 0\n\t" \
    "buffer_load_dwordx4 v224, %[rs], %[st] offen offset:0 lds\n\t" \
    "buffer_load_dwordx4 v225, %[rs], %[st] offen offset:1024 lds\n\t" \
    "buffer_load_dwordx4 v226, %[rs], %[st] offen offset:2048 lds\n\t" \
    "buffer_load_dwordx4 v227, %[rs], %[st] offen offset:3072 lds\n\t" \
    "s_mul_i32 %[st], %[rstep], 2\n\t" \
    "s_add_u32 m0, %[ml], 8192\n\t" \
    "s_nop 0\n\t" \
    "buffer_load_dwordx4 v224, %[rs], %[st] offen offset:0 lds\n\t" \
    "buffer_load_dwordx4 v225, %[rs], %[st] offen offset:1024 lds\n\t" \
    "buffer_load_dwordx4 v226, %[rs], %[st] offen offset:2048 lds\n\t" \
    "buffer_load_dwordx4 v227, %[rs], %[st] offen offset:3072 lds\n\t" \
    "s_mul_i32 %[st], %[rstep], 3\n\t" \
    "s_add_u32 m0, %[ml], 12288\n\t" \
    "s_nop 0\n\t" \
    "buffer_load_dwordx4 v224, %[rs], %[st] offen offset:0 lds\n\t" \
    "buffer_load_dwordx4 v225, %[rs], %[st] offen offset:1024 lds\n\t" \
    "buffer_load_dwordx4 v226, %[rs], %[st] offen offset:2048 lds\n\t" \
    "buffer_load_dwordx4 v227, %[rs], %[st] offen offset:3072 lds\n\t" \
    "s_mov_b32 m0, %[sv]\n\t"
#define E_MAIN_ASM \
    "s_mov_b32 %[st], 163840\n\t" \
    "buffer_load_dwordx4 v[80:83], v232, %[rh], %[st] offen offset:0 sc1\n\t" \
    "buffer_load_dwordx4 v[84:87], v232, %[rh], %[st] offen offset:1024 sc1\n\t" \
    "buffer_load_dwordx4 v[88:91], v232, %[rh], %[st] offen offset:2048 sc1\n\t" \
    "buffer_load_dwordx4 v[92:95], v232, %[rh], %[st] offen offset:3072 sc1\n\t" \
    "s_add_u32 %[st], %[st], 4096\n\t" \
    "buffer_load_dwordx4 v[96:99], v232, %[rh], %[st] offen offset:0 sc1\n\t" \
    "s_mov_b32 %[st], 204800\n\t" \
    "buffer_load_dwordx4 v[100:103], v232, %[rh], %[st] offen offset:0 sc1\n\t" \
    "buffer_load_dwordx4 v[104:107], v232, %[rh], %[st] offen offset:1024 sc1\n\t" \
    "buffer_load_dwordx4 v[108:111], v232, %[rh], %[st] offen offset:2048 sc1\n\t" \
    "buffer_load_dwordx4 v[112:115], v232, %[rh], %[st] offen offset:3072 sc1\n\t" \
    "s_add_u32 %[st], %[st], 4096\n\t" \
    "buffer_load_dwordx4 v[116:119], v232, %[rh], %[st] offen offset:0 sc1\n\t" \
    "s_mov_b32 %[st], 0\n\t" \
    "buffer_load_dwordx4 v[120:123], v232, %[rh], %[st] offen offset:0 sc1\n\t" \
    "buffer_load_dwordx4 v[124:127], v232, %[rh], %[st] offen offset:1024 sc1\n\t" \
    "buffer_load_dwordx4 v[128:131], v232, %[rh], %[st] offen offset:2048 sc1\n\t" \
    "buffer_load_dwordx4 v[132:135], v232, %[rh], %[st] offen offset:3072 sc1\n\t" \
    "s_add_u32 %[st], %[st], 4096\n\t" \
    "buffer_load_dwordx4 v[136:139], v232, %[rh], %[st] offen offset:0 sc1\n\t" \
    "s_mov_b32 %[st], 40960\n\t" \
    "buffer_load_dwordx4 v[140:143], v232, %[rh], %[st] offen offset:0 sc1\n\t" \
    "buffer_load_dwordx4 v[144:147], v232, %[rh], %[st] offen offset:1024 sc1\n\t" \
    "buffer_load_dwordx4 v[148:151], v232, %[rh], %[st] offen offset:2048 sc1\n\t" \
    "buffer_load_dwordx4 v[152:155], v232, %[rh], %[st] offen offset:3072 sc1\n\t" \
    "s_add_u32 %[st], %[st], 4096\n\t" \
    "buffer_load_dwordx4 v[156:159], v232, %[rh], %[st] offen offset:0 sc1\n\t" \
    "v_mov_b32 v0, 0\n\t" \
    "v_mov_b32 v1, 0\n\t" \
    "v_mov_b32 v2, 0\n\t" \
    "v_mov_b32 v3, 0\n\t" \
    "v_mov_b32 v4, 0\n\t" \
    "v_mov_b32 v5, 0\n\t" \
    "v_mov_b32 v6, 0\n\t" \
    "v_mov_b32 v7, 0\n\t" \
    "v_mov_b32 v8, 0\n\t" \
    "v_mov_b32 v9, 0\n\t" \
    "v_mov_b32 v10, 0\n\t" \
    "v_mov_b32 v11, 0\n\t" \
    "v_mov_b32 v12, 0\n\t" \
    "v_mov_b32 v13, 0\n\t" \
    "v_mov_b32 v14, 0\n\t" \
    "v_mov_b32 v15, 0\n\t" \
    "v_mov_b32 v16, 0\n\t" \
    "v_mov_b32 v17, 0\n\t" \
    "v_mov_b32 v18, 0\n\t" \
    "v_mov_b32 v19, 0\n\t" \
    "v_mov_b32 v20, 0\n\t" \
    "v_mov_b32 v21, 0\n\t" \
    "v_mov_b32 v22, 0\n\t" \
    "v_mov_b32 v23, 0\n\t" \
    "v_mov_b32 v24, 0\n\t" \
    "v_mov_b32 v25, 0\n\t" \
    "v_mov_b32 v26, 0\n\t" \
    "v_mov_b32 v27, 0\n\t" \
    "v_mov_b32 v28, 0\n\t" \
    "v_mov_b32 v29, 0\n\t" \
    "v_mov_b32 v30, 0\n\t" \
    "v_mov_b32 v31, 0\n\t" \
    "v_mov_b32 v32, 0\n\t" \
    "v_mov_b32 v33, 0\n\t" \
    "v_mov_b32 v34, 0\n\t" \
    "v_mov_b32 v35, 0\n\t" \
    "v_mov_b32 v36, 0\n\t" \
    "v_mov_b32 v37, 0\n\t" \
    "v_mov_b32 v38, 0\n\t" \
    "v_mov_b32 v39, 0\n\t" \
    "v_mov_b32 v40, 0\n\t" \
    "v_mov_b32 v41, 0\n\t" \
    "v_mov_b32 v42, 0\n\t" \
    "v_mov_b32 v43, 0\n\t" \
    "v_mov_b32 v44, 0\n\t" \
    "v_mov_b32 v45, 0\n\t" \
    "v_mov_b32 v46, 0\n\t" \
    "v_mov_b32 v47, 0\n\t" \
    "v_mov_b32 v48, 0\n\t" \
    "v_mov_b32 v49, 0\n\t" \
    "v_mov_b32 v50, 0\n\t" \
    "v_mov_b32 v51, 0\n\t" \
    "v_mov_b32 v52, 0\n\t" \
    "v_mov_b32 v53, 0\n\t" \
    "v_mov_b32 v54, 0\n\t" \
    "v_mov_b32 v55, 0\n\t" \
    "v_mov_b32 v56, 0\n\t" \
    "v_mov_b32 v57, 0\n\t" \
    "v_mov_b32 v58, 0\n\t" \
    "v_mov_b32 v59, 0\n\t" \
    "v_mov_b32 v60, 0\n\t" \
    "v_mov_b32 v61, 0\n\t" \
    "v_mov_b32 v62, 0\n\t" \
    "v_mov_b32 v63, 0\n\t" \
    "v_mov_b32 v64, 0\n\t" \
    "v_mov_b32 v65, 0\n\t" \
    "v_mov_b32 v66, 0\n\t" \
    "v_mov_b32 v67, 0\n\t" \
    "v_mov_b32 v68, 0\n\t" \
    "v_mov_b32 v69, 0\n\t" \
    "v_mov_b32 v70, 0\n\t" \
    "v_mov_b32 v71, 0\n\t" \
    "v_mov_b32 v72, 0\n\t" \
    "v_mov_b32 v73, 0\n\t" \
    "v_mov_b32 v74, 0\n\t" \
    "v_mov_b32 v75, 0\n\t" \
    "v_mov_b32 v76, 0\n\t" \
    "v_mov_b32 v77, 0\n\t" \
    "v_mov_b32 v78, 0\n\t" \
    "v_mov_b32 v79, 0\n\t" \
    "s_waitcnt vmcnt(15)\n\t" \
    "v_mfma_f32_16x16x32_bf16 v[0:3], v[80:83], v[160:163], v[0:3]\n\t" \
    "v_mfma_f32_16x16x32_bf16 v[4:7], v[84:87], v[160:163], v[4:7]\n\t" \
    "v_mfma_f32_16x16x32_bf16 v[8:11], v[88:91], v[160:163], v[8:11]\n\t" \
    "v_mfma_f32_16x16x32_bf16 v[12:15], v[92:95], v[160:163], v[12:15]\n\t" \
    "v_mfma_f32_16x16x32_bf16 v[16:19], v[96:99], v[160:163], v[16:19]\n\t" \
    "v_mfma_f32_16x16x32_bf16 v[20:23], v[80:83], v[164:167], v[20:23]\n\t" \
    "v_mfma_f32_16x16x32_bf16 v[24:27], v[84:87], v[164:167], v[24:27]\n\t" \
    "v_mfma_f32_16x16x32_bf16 v[28:31], v[88:91], v[164:167], v[28:31]\n\t" \
    "v_mfma_f32_16x16x32_bf16 v[32:35], v[92:95], v[164:167], v[32:35]\n\t" \
    "v_mfma_f32_16x16x32_bf16 v[36:39], v[96:99], v[164:167], v[36:39]\n\t" \
    "v_mfma_f32_16x16x32_bf16 v[40:43], v[80:83], v[168:171], v[40:43]\n\t" \
    "v_mfma_f32_16x16x32_bf16 v[44:47], v[84:87], v[168:171], v[44:47]\n\t" \
    "v_mfma_f32_16x16x32_bf16 v[48:51], v[88:91], v[168:171], v[48:51]\n\t" \
    "v_mfma_f32_16x16x32_bf16 v[52:55], v[92:95], v[168:171], v[52:55]\n\t" \
    "v_mfma_f32_16x16x32_bf16 v[56:59], v[96:99], v[168:171], v[56:59]\n\t" \
    "v_mfma_f32_16x16x32_bf16 v[60:63], v[80:83], v[172:175], v[60:63]\n\t" \
    "v_mfma_f32_16x16x32_bf16 v[64:67], v[84:87], v[172:175], v[64:67]\n\t" \
    "v_mfma_f32_16x16x32_bf16 v[68:71], v[88:91], v[172:175], v[68:71]\n\t" \
    "v_mfma_f32_16x16x32_bf16 v[72:75], v[92:95], v[172:175], v[72:75]\n\t" \
    "v_mfma_f32_16x16x32_bf16 v[76:79], v[96:99], v[172:175], v[76:79]\n\t" \
    "s_mov_b32 %[st], 81920\n\t" \
    "buffer_load_dwordx4 v[80:83], v232, %[rh], %[st] offen offset:0 sc1\n\t" \
    "buffer_load_dwordx4 v[84:87], v232, %[rh], %[st] offen offset:1024 sc1\n\t" \
    "buffer_load_dwordx4 v[88:91], v232, %[rh], %[st] offen offset:2048 sc1\n\t" \
    "buffer_load_dwordx4 v[92:95], v232, %[rh], %[st] offen offset:3072 sc1\n\t" \
    "s_add_u32 %[st], %[st], 4096\n\t" \
    "buffer_load_dwordx4 v[96:99], v232, %[rh], %[st] offen offset:0 sc1\n\t" \
    "s_waitcnt vmcnt(15)\n\t" \
    "v_mfma_f32_16x16x32_bf16 v[0:3], v[100:103], v[176:179], v[0:3]\n\t" \
    "ds_read_b64_tr_b16 v[192:193], v228 offset:0\n\t" \
    "v_mfma_f32_16x16x32_bf16 v[4:7], v[104:107], v[176:179], v[4:7]\n\t" \
    "v_mfma_f32_16x16x32_bf16 v[8:11], v[108:111], v[176:179], v[8:11]\n\t" \
    "ds_read_b64_tr_b16 v[194:195], v228 offset:1024\n\t" \
    "v_mfma_f32_16x16x32_bf16 v[12:15], v[112:115], v[176:179], v[12:15]\n\t" \
    "v_mfma_f32_16x16x32_bf16 v[16:19], v[116:119], v[176:179], v[16:19]\n\t" \
    "v_mfma_f32_16x16x32_bf16 v[20:23], v[100:103], v[180:183], v[20:23]\n\t" \
    "ds_read_b64_tr_b16 v[196:197], v229 offset:0\n\t" \
    "v_mfma_f32_16x16x32_bf16 v[24:27], v[104:107], v[180:183], v[24:27]\n\t" \
    "v_mfma_f32_16x16x32_bf16 v[28:31], v[108:111], v[180:183], v[28:31]\n\t" \
    "ds_read_b64_tr_b16 v[198:199], v229 offset:1024\n\t" \
    "v_mfma_f32_16x16x32_bf16 v[32:35], v[112:115], v[180:183], v[32:35]\n\t" \
    "v_mfma_f32_16x16x32_bf16 v[36:39], v[116:119], v[180:183], v[36:39]\n\t" \
    "v_mfma_f32_16x16x32_bf16 v[40:43], v[100:103], v[184:187], v[40:43]\n\t" \
    "ds_read_b64_tr_b16 v[200:201], v230 offset:0\n\t" \
    "v_mfma_f32_16x16x32_bf16 v[44:47], v[104:107], v[184:187], v[44:47]\n\t" \
    "v_mfma_f32_16x16x32_bf16 v[48:51], v[108:111], v[184:187], v[48:51]\n\t" \
    "ds_read_b64_tr_b16 v[202:203], v230 offset:1024\n\t" \
    "v_mfma_f32_16x16x32_bf16 v[52:55], v[112:115], v[184:187], v[52:55]\n\t" \
    "v_mfma_f32_16x16x32_bf16 v[56:59], v[116:119], v[184:187], v[56:59]\n\t" \
    "v_mfma_f32_16x16x32_bf16 v[60:63], v[100:103], v[188:191], v[60:63]\n\t" \
    "ds_read_b64_tr_b16 v[204:205], v231 offset:0\n\t" \
    "v_mfma_f32_16x16x32_bf16 v[64:67], v[104:107], v[188:191], v[64:67]\n\t" \
    "v_mfma_f32_16x16x32_bf16 v[68:71], v[108:111], v[188:191], v[68:71]\n\t" \
    "ds_read_b64_tr_b16 v[206:207], v231 offset:1024\n\t" \
    "v_mfma_f32_16x16x32_bf16 v[72:75], v[112:115], v[188:191], v[72:75]\n\t" \
    "v_mfma_f32_16x16x32_bf16 v[76:79], v[116:119], v[188:191], v[76:79]\n\t" \
    "s_mov_b32 %[st], 122880\n\t" \
    "buffer_load_dwordx4 v[100:103], v232, %[rh], %[st] offen offset:0 sc1\n\t" \
    "buffer_load_dwordx4 v[104:107], v232, %[rh], %[st] offen offset:1024 sc1\n\t" \
    "buffer_load_dwordx4 v[108:111], v232, %[rh], %[st] offen offset:2048 sc1\n\t" \
    "buffer_load_dwordx4 v[112:115], v232, %[rh], %[st] offen offset:3072 sc1\n\t" \
    "s_add_u32 %[st], %[st], 4096\n\t" \
    "buffer_load_dwordx4 v[116:119], v232, %[rh], %[st] offen offset:0 sc1\n\t" \
    "s_waitcnt vmcnt(15) lgkmcnt(0)\n\t" \
    "v_mfma_f32_16x16x32_bf16 v[0:3], v[120:123], v[192:195], v[0:3]\n\t" \
    "ds_read_b64_tr_b16 v[208:209], v228 offset:4096\n\t" \
    "v_mfma_f32_16x16x32_bf16 v[4:7], v[124:127], v[192:195], v[4:7]\n\t" \
    "v_mfma_f32_16x16x32_bf16 v[8:11], v[128:131], v[192:195], v[8:11]\n\t" \
    "ds_read_b64_tr_b16 v[210:211], v228 offset:5120\n\t" \
    "v_mfma_f32_16x16x32_bf16 v[12:15], v[132:135], v[192:195], v[12:15]\n\t" \
    "v_mfma_f32_16x16x32_bf16 v[16:19], v[136:139], v[192:195], v[16:19]\n\t" \
    "v_mfma_f32_16x16x32_bf16 v[20:23], v[120:123], v[196:199], v[20:23]\n\t" \
    "ds_read_b64_tr_b16 v[212:213], v229 offset:4096\n\t" \
    "v_mfma_f32_16x16x32_bf16 v[24:27], v[124:127], v[196:199], v[24:27]\n\t" \
    "v_mfma_f32_16x16x32_bf16 v[28:31], v[128:131], v[196:199], v[28:31]\n\t" \
    "ds_read_b64_tr_b16 v[214:215], v229 offset:5120\n\t" \
    "v_mfma_f32_16x16x32_bf16 v[32:35], v[132:135], v[196:199], v[32:35]\n\t" \
    "v_mfma_f32_16x16x32_bf16 v[36:39], v[136:139], v[196:199], v[36:39]\n\t" \
    "v_mfma_f32_16x16x32_bf16 v[40:43], v[120:123], v[200:203], v[40:43]\n\t" \
    "ds_read_b64_tr_b16 v[216:217], v230 offset:4096\n\t" \
    "v_mfma_f32_16x16x32_bf16 v[44:47], v[124:127], v[200:203], v[44:47]\n\t" \
    "v_mfma_f32_16x16x32_bf16 v[48:51], v[128:131], v[200:203], v[48:51]\n\t" \
    "ds_read_b64_tr_b16 v[218:219], v230 offset:5120\n\t" \
    "v_mfma_f32_16x16x32_bf16 v[52:55], v[132:135], v[200:203], v[52:55]\n\t" \
    "v_mfma_f32_16x16x32_bf16 v[56:59], v[136:139], v[200:203], v[56:59]\n\t" \
    "v_mfma_f32_16x16x32_bf16 v[60:63], v[120:123], v[204:207], v[60:63]\n\t" \
    "ds_read_b64_tr_b16 v[220:221], v231 offset:4096\n\t" \
    "v_mfma_f32_16x16x32_bf16 v[64:67], v[124:127], v[204:207], v[64:67]\n\t" \
    "v_mfma_f32_16x16x32_bf16 v[68:71], v[128:131], v[204:207], v[68:71]\n\t" \
    "ds_read_b64_tr_b16 v[222:223], v231 offset:5120\n\t" \
    "v_mfma_f32_16x16x32_bf16 v[72:75], v[132:135], v[204:207], v[72:75]\n\t" \
    "v_mfma_f32_16x16x32_bf16 v[76:79], v[136:139], v[204:207], v[76:79]\n\t" \
    "s_waitcnt vmcnt(10) lgkmcnt(0)\n\t" \
    "v_mfma_f32_16x16x32_bf16 v[0:3], v[140:143], v[208:211], v[0:3]\n\t" \
    "ds_read_b64_tr_b16 v[192:193], v228 offset:8192\n\t" \
    "v_mfma_f32_16x16x32_bf16 v[4:7], v[144:147], v[208:211], v[4:7]\n\t" \
    "v_mfma_f32_16x16x32_bf16 v[8:11], v[148:151], v[208:211], v[8:11]\n\t" \
    "ds_read_b64_tr_b16 v[194:195], v228 offset:9216\n\t" \
    "v_mfma_f32_16x16x32_bf16 v[12:15], v[152:155], v[208:211], v[12:15]\n\t" \
    "v_mfma_f32_16x16x32_bf16 v[16:19], v[156:159], v[208:211], v[16:19]\n\t" \
    "v_mfma_f32_16x16x32_bf16 v[20:23], v[140:143], v[212:215], v[20:23]\n\t" \
    "ds_read_b64_tr_b16 v[196:197], v229 offset:8192\n\t" \
    "v_mfma_f32_16x16x32_bf16 v[24:27], v[144:147], v[212:215], v[24:27]\n\t" \
    "v_mfma_f32_16x16x32_bf16 v[28:31], v[148:151], v[212:215], v[28:31]\n\t" \
    "ds_read_b64_tr_b16 v[198:199], v229 offset:9216\n\t" \
    "v_mfma_f32_16x16x32_bf16 v[32:35], v[152:155], v[212:215], v[32:35]\n\t" \
    "v_mfma_f32_16x16x32_bf16 v[36:39], v[156:159], v[212:215], v[36:39]\n\t" \
    "v_mfma_f32_16x16x32_bf16 v[40:43], v[140:143], v[216:219], v[40:43]\n\t" \
    "ds_read_b64_tr_b16 v[200:201], v230 offset:8192\n\t" \
    "v_mfma_f32_16x16x32_bf16 v[44:47], v[144:147], v[216:219], v[44:47]\n\t" \
    "v_mfma_f32_16x16x32_bf16 v[48:51], v[148:151], v[216:219], v[48:51]\n\t" \
    "ds_read_b64_tr_b16 v[202:203], v230 offset:9216\n\t" \
    "v_mfma_f32_16x16x32_bf16 v[52:55], v[152:155], v[216:219], v[52:55]\n\t" \
    "v_mfma_f32_16x16x32_bf16 v[56:59], v[156:159], v[216:219], v[56:59]\n\t" \
    "v_mfma_f32_16x16x32_bf16 v[60:63], v[140:143], v[220:223], v[60:63]\n\t" \
    "ds_read_b64_tr_b16 v[204:205], v231 offset:8192\n\t" \
    "v_mfma_f32_16x16x32_bf16 v[64:67], v[144:147], v[220:223], v[64:67]\n\t" \
    "v_mfma_f32_16x16x32_bf16 v[68:71], v[148:151], v[220:223], v[68:71]\n\t" \
    "ds_read_b64_tr_b16 v[206:207], v231 offset:9216\n\t" \
    "v_mfma_f32_16x16x32_bf16 v[72:75], v[152:155], v[220:223], v[72:75]\n\t" \
    "v_mfma_f32_16x16x32_bf16 v[76:79], v[156:159], v[220:223], v[76:79]\n\t" \
    "s_waitcnt vmcnt(5) lgkmcnt(0)\n\t" \
    "v_mfma_f32_16x16x32_bf16 v[0:3], v[80:83], v[192:195], v[0:3]\n\t" \
    "ds_read_b64_tr_b16 v[208:209], v228 offset:12288\n\t" \
    "v_mfma_f32_16x16x32_bf16 v[4:7], v[84:87], v[192:195], v[4:7]\n\t" \
    "v_mfma_f32_16x16x32_bf16 v[8:11], v[88:91], v[192:195], v[8:11]\n\t" \
    "ds_read_b64_tr_b16 v[210:211], v228 offset:13312\n\t" \
    "v_mfma_f32_16x16x32_bf16 v[12:15], v[92:95], v[192:195], v[12:15]\n\t" \
    "v_mfma_f32_16x16x32_bf16 v[16:19], v[96:99], v[192:195], v[16:19]\n\t" \
    "v_mfma_f32_16x16x32_bf16 v[20:23], v[80:83], v[196:199], v[20:23]\n\t" \
    "ds_read_b64_tr_b16 v[212:213], v229 offset:12288\n\t" \
    "v_mfma_f32_16x16x32_bf16 v[24:27], v[84:87], v[196:199], v[24:27]\n\t" \
    "v_mfma_f32_16x16x32_bf16 v[28:31], v[88:91], v[196:199], v[28:31]\n\t" \
    "ds_read_b64_tr_b16 v[214:215], v229 offset:13312\n\t" \
    "v_mfma_f32_16x16x32_bf16 v[32:35], v[92:95], v[196:199], v[32:35]\n\t" \
    "v_mfma_f32_16x16x32_bf16 v[36:39], v[96:99], v[196:199], v[36:39]\n\t" \
    "v_mfma_f32_16x16x32_bf16 v[40:43], v[80:83], v[200:203], v[40:43]\n\t" \
    "ds_read_b64_tr_b16 v[216:217], v230 offset:12288\n\t" \
    "v_mfma_f32_16x16x32_bf16 v[44:47], v[84:87], v[200:203], v[44:47]\n\t" \
    "v_mfma_f32_16x16x32_bf16 v[48:51], v[88:91], v[200:203], v[48:51]\n\t" \
    "ds_read_b64_tr_b16 v[218:219], v230 offset:13312\n\t" \
    "v_mfma_f32_16x16x32_bf16 v[52:55], v[92:95], v[200:203], v[52:55]\n\t" \
    "v_mfma_f32_16x16x32_bf16 v[56:59], v[96:99], v[200:203], v[56:59]\n\t" \
    "v_mfma_f32_16x16x32_bf16 v[60:63], v[80:83], v[204:207], v[60:63]\n\t" \
    "ds_read_b64_tr_b16 v[220:221], v231 offset:12288\n\t" \
    "v_mfma_f32_16x16x32_bf16 v[64:67], v[84:87], v[204:207], v[64:67]\n\t" \
    "v_mfma_f32_16x16x32_bf16 v[68:71], v[88:91], v[204:207], v[68:71]\n\t" \
    "ds_read_b64_tr_b16 v[222:223], v231 offset:13312\n\t" \
    "v_mfma_f32_16x16x32_bf16 v[72:75], v[92:95], v[204:207], v[72:75]\n\t" \
    "v_mfma_f32_16x16x32_bf16 v[76:79], v[96:99], v[204:207], v[76:79]\n\t" \
    "s_waitcnt vmcnt(0) lgkmcnt(0)\n\t" \
    "v_mfma_f32_16x16x32_bf16 v[0:3], v[100:103], v[208:211], v[0:3]\n\t" \
    "v_mfma_f32_16x16x32_bf16 v[4:7], v[104:107], v[208:211], v[4:7]\n\t" \
    "v_mfma_f32_16x16x32_bf16 v[8:11], v[108:111], v[208:211], v[8:11]\n\t" \
    "v_mfma_f32_16x16x32_bf16 v[12:15], v[112:115], v[208:211], v[12:15]\n\t" \
    "v_mfma_f32_16x16x32_bf16 v[16:19], v[116:119], v[208:211], v[16:19]\n\t" \
    "v_mfma_f32_16x16x32_bf16 v[20:23], v[100:103], v[212:215], v[20:23]\n\t" \
    "v_mfma_f32_16x16x32_bf16 v[24:27], v[104:107], v[212:215], v[24:27]\n\t" \
    "v_mfma_f32_16x16x32_bf16 v[28:31], v[108:111], v[212:215], v[28:31]\n\t" \
    "v_mfma_f32_16x16x32_bf16 v[32:35], v[112:115], v[212:215], v[32:35]\n\t" \
    "v_mfma_f32_16x16x32_bf16 v[36:39], v[116:119], v[212:215], v[36:39]\n\t" \
    "v_mfma_f32_16x16x32_bf16 v[40:43], v[100:103], v[216:219], v[40:43]\n\t" \
    "v_mfma_f32_16x16x32_bf16 v[44:47], v[104:107], v[216:219], v[44:47]\n\t" \
    "v_mfma_f32_16x16x32_bf16 v[48:51], v[108:111], v[216:219], v[48:51]\n\t" \
    "v_mfma_f32_16x16x32_bf16 v[52:55], v[112:115], v[216:219], v[52:55]\n\t" \
    "v_mfma_f32_16x16x32_bf16 v[56:59], v[116:119], v[216:219], v[56:59]\n\t" \
    "v_mfma_f32_16x16x32_bf16 v[60:63], v[100:103], v[220:223], v[60:63]\n\t" \
    "v_mfma_f32_16x16x32_bf16 v[64:67], v[104:107], v[220:223], v[64:67]\n\t" \
    "v_mfma_f32_16x16x32_bf16 v[68:71], v[108:111], v[220:223], v[68:71]\n\t" \
    "v_mfma_f32_16x16x32_bf16 v[72:75], v[112:115], v[220:223], v[72:75]\n\t" \
    "v_mfma_f32_16x16x32_bf16 v[76:79], v[116:119], v[220:223], v[76:79]\n\t" \
    "s_nop 7\n\t"
#define C_PRE_ASM \
    "ds_read_b64_tr_b16 v[60:61], v220 offset:0\n\t" \
    "ds_read_b64_tr_b16 v[62:63], v220 offset:2560\n\t" \
    "ds_read_b64_tr_b16 v[64:65], v220 offset:32\n\t" \
    "ds_read_b64_tr_b16 v[66:67], v220 offset:2592\n\t" \
    "ds_read_b64_tr_b16 v[68:69], v220 offset:64\n\t" \
    "ds_read_b64_tr_b16 v[70:71], v220 offset:2624\n\t" \
    "ds_read_b64_tr_b16 v[72:73], v220 offset:96\n\t" \
    "ds_read_b64_tr_b16 v[74:75], v220 offset:2656\n\t" \
    "ds_read_b64_tr_b16 v[76:77], v220 offset:128\n\t" \
    "ds_read_b64_tr_b16 v[78:79], v220 offset:2688\n\t" \
    "ds_read_b64_tr_b16 v[80:81], v220 offset:5120\n\t" \
    "ds_read_b64_tr_b16 v[82:83], v220 offset:7680\n\t" \
    "ds_read_b64_tr_b16 v[84:85], v220 offset:5152\n\t" \
    "ds_read_b64_tr_b16 v[86:87], v220 offset:7712\n\t" \
    "ds_read_b64_tr_b16 v[88:89], v220 offset:5184\n\t" \
    "ds_read_b64_tr_b16 v[90:91], v220 offset:7744\n\t" \
    "ds_read_b64_tr_b16 v[92:93], v220 offset:5216\n\t" \
    "ds_read_b64_tr_b16 v[94:95], v220 offset:7776\n\t" \
    "ds_read_b64_tr_b16 v[96:97], v220 offset:5248\n\t" \
    "ds_read_b64_tr_b16 v[98:99], v220 offset:7808\n\t" \
    "ds_read_b64_tr_b16 v[100:101], v220 offset:10240\n\t" \
    "ds_read_b64_tr_b16 v[102:103], v220 offset:12800\n\t" \
    "ds_read_b64_tr_b16 v[104:105], v220 offset:10272\n\t" \
    "ds_read_b64_tr_b16 v[106:107], v220 offset:12832\n\t" \
    "ds_read_b64_tr_b16 v[108:109], v220 offset:10304\n\t" \
    "ds_read_b64_tr_b16 v[110:111], v220 offset:12864\n\t" \
    "ds_read_b64_tr_b16 v[112:113], v220 offset:10336\n\t" \
    "ds_read_b64_tr_b16 v[114:115], v220 offset:12896\n\t" \
    "ds_read_b64_tr_b16 v[116:117], v220 offset:10368\n\t" \
    "ds_read_b64_tr_b16 v[118:119], v220 offset:12928\n\t" \
    "ds_read_b64_tr_b16 v[120:121], v220 offset:15360\n\t" \
    "ds_read_b64_tr_b16 v[122:123], v220 offset:17920\n\t" \
    "ds_read_b64_tr_b16 v[124:125], v220 offset:15392\n\t" \
    "ds_read_b64_tr_b16 v[126:127], v220 offset:17952\n\t" \
    "ds_read_b64_tr_b16 v[128:129], v220 offset:15424\n\t" \
    "ds_read_b64_tr_b16 v[130:131], v220 offset:17984\n\t" \
    "ds_read_b64_tr_b16 v[132:133], v220 offset:15456\n\t" \
    "ds_read_b64_tr_b16 v[134:135], v220 offset:18016\n\t" \
    "ds_read_b64_tr_b16 v[136:137], v220 offset:15488\n\t" \
    "ds_read_b64_tr_b16 v[138:139], v220 offset:18048\n\t" \
    "ds_read_b64_tr_b16 v[140:141], v220 offset:20480\n\t" \
    "ds_read_b64_tr_b16 v[142:143], v220 offset:23040\n\t" \
    "ds_read_b64_tr_b16 v[144:145], v220 offset:20512\n\t" \
    "ds_read_b64_tr_b16 v[146:147], v220 offset:23072\n\t" \
    "ds_read_b64_tr_b16 v[148:149], v220 offset:20544\n\t" \
    "ds_read_b64_tr_b16 v[150:151], v220 offset:23104\n\t" \
    "ds_read_b64_tr_b16 v[152:153], v220 offset:20576\n\t" \
    "ds_read_b64_tr_b16 v[154:155], v220 offset:23136\n\t" \
    "ds_read_b64_tr_b16 v[156:157], v220 offset:20608\n\t" \
    "ds_read_b64_tr_b16 v[158:159], v220 offset:23168\n\t" \
    "s_waitcnt lgkmcnt(0)\n\t"
#define C_MAIN_ASM \
    "s_mov_b32 %[st], 0\n\t" \
    "buffer_load_dwordx4 v[20:23], v221, %[rh], %[st] offen offset:0 sc1\n\t" \
    "buffer_load_dwordx4 v[24:27], v221, %[rh], %[st] offen offset:1024 sc1\n\t" \
    "buffer_load_dwordx4 v[28:31], v221, %[rh], %[st] offen offset:2048 sc1\n\t" \
    "buffer_load_dwordx4 v[32:35], v221, %[rh], %[st] offen offset:3072 sc1\n\t" \
    "s_mov_b32 %[st], 4096\n\t" \
    "buffer_load_dwordx4 v[36:39], v221, %[rh], %[st] offen offset:0 sc1\n\t" \
    "buffer_load_dwordx4 v[40:43], v221, %[rh], %[st] offen offset:1024 sc1\n\t" \
    "buffer_load_dwordx4 v[44:47], v221, %[rh], %[st] offen offset:2048 sc1\n\t" \
    "buffer_load_dwordx4 v[48:51], v221, %[rh], %[st] offen offset:3072 sc1\n\t" \
    "s_mov_b32 %[st], 8192\n\t" \
    "buffer_load_dwordx4 v[52:55], v221, %[rh], %[st] offen offset:0 sc1\n\t" \
    "buffer_load_dwordx4 v[56:59], v221, %[rh], %[st] offen offset:1024 sc1\n\t" \
    "v_mov_b32 v0, 0\n\t" \
    "v_mov_b32 v1, 0\n\t" \
    "v_mov_b32 v2, 0\n\t" \
    "v_mov_b32 v3, 0\n\t" \
    "v_mov_b32 v4, 0\n\t" \
    "v_mov_b32 v5, 0\n\t" \
    "v_mov_b32 v6, 0\n\t" \
    "v_mov_b32 v7, 0\n\t" \
    "v_mov_b32 v8, 0\n\t" \
    "v_mov_b32 v9, 0\n\t" \
    "v_mov_b32 v10, 0\n\t" \
    "v_mov_b32 v11, 0\n\t" \
    "v_mov_b32 v12, 0\n\t" \
    "v_mov_b32 v13, 0\n\t" \
    "v_mov_b32 v14, 0\n\t" \
    "v_mov_b32 v15, 0\n\t" \
    "v_mov_b32 v16, 0\n\t" \
    "v_mov_b32 v17, 0\n\t" \
    "v_mov_b32 v18, 0\n\t" \
    "v_mov_b32 v19, 0\n\t" \
    "ds_read_b64_tr_b16 v[160:161], v220 offset:25600\n\t" \
    "ds_read_b64_tr_b16 v[162:163], v220 offset:28160\n\t" \
    "ds_read_b64_tr_b16 v[164:165], v220 offset:25632\n\t" \
    "ds_read_b64_tr_b16 v[166:167], v220 offset:28192\n\t" \
    "ds_read_b64_tr_b16 v[168:169], v220 offset:25664\n\t" \
    "ds_read_b64_tr_b16 v[170:171], v220 offset:28224\n\t" \
    "ds_read_b64_tr_b16 v[172:173], v220 offset:25696\n\t" \
    "ds_read_b64_tr_b16 v[174:175], v220 offset:28256\n\t" \
    "ds_read_b64_tr_b16 v[176:177], v220 offset:25728\n\t" \
    "ds_read_b64_tr_b16 v[178:179], v220 offset:28288\n\t" \
    "ds_read_b64_tr_b16 v[180:181], v220 offset:30720\n\t" \
    "ds_read_b64_tr_b16 v[182:183], v220 offset:33280\n\t" \
    "ds_read_b64_tr_b16 v[184:185], v220 offset:30752\n\t" \
    "ds_read_b64_tr_b16 v[186:187], v220 offset:33312\n\t" \
    "ds_read_b64_tr_b16 v[188:189], v220 offset:30784\n\t" \
    "ds_read_b64_tr_b16 v[190:191], v220 offset:33344\n\t" \
    "ds_read_b64_tr_b16 v[192:193], v220 offset:30816\n\t" \
    "ds_read_b64_tr_b16 v[194:195], v220 offset:33376\n\t" \
    "ds_read_b64_tr_b16 v[196:197], v220 offset:30848\n\t" \
    "ds_read_b64_tr_b16 v[198:199], v220 offset:33408\n\t" \
    "ds_read_b64_tr_b16 v[200:201], v220 offset:35840\n\t" \
    "ds_read_b64_tr_b16 v[202:203], v220 offset:38400\n\t" \
    "ds_read_b64_tr_b16 v[204:205], v220 offset:35872\n\t" \
    "ds_read_b64_tr_b16 v[206:207], v220 offset:38432\n\t" \
    "ds_read_b64_tr_b16 v[208:209], v220 offset:35904\n\t" \
    "ds_read_b64_tr_b16 v[210:211], v220 offset:38464\n\t" \
    "ds_read_b64_tr_b16 v[212:213], v220 offset:35936\n\t" \
    "ds_read_b64_tr_b16 v[214:215], v220 offset:38496\n\t" \
    "ds_read_b64_tr_b16 v[216:217], v220 offset:35968\n\t" \
    "ds_read_b64_tr_b16 v[218:219], v220 offset:38528\n\t" \
    "s_waitcnt vmcnt(9)\n\t" \
    "v_mfma_f32_16x16x32_bf16 v[0:3], v[60:63], v[20:23], v[0:3]\n\t" \
    "v_mfma_f32_16x16x32_bf16 v[4:7], v[64:67], v[20:23], v[4:7]\n\t" \
    "v_mfma_f32_16x16x32_bf16 v[8:11], v[68:71], v[20:23], v[8:11]\n\t" \
    "v_mfma_f32_16x16x32_bf16 v[12:15], v[72:75], v[20:23], v[12:15]\n\t" \
    "v_mfma_f32_16x16x32_bf16 v[16:19], v[76:79], v[20:23], v[16:19]\n\t" \
    "s_waitcnt vmcnt(8)\n\t" \
    "v_mfma_f32_16x16x32_bf16 v[0:3], v[80:83], v[24:27], v[0:3]\n\t" \
    "v_mfma_f32_16x16x32_bf16 v[4:7], v[84:87], v[24:27], v[4:7]\n\t" \
    "v_mfma_f32_16x16x32_bf16 v[8:11], v[88:91], v[24:27], v[8:11]\n\t" \
    "v_mfma_f32_16x16x32_bf16 v[12:15], v[92:95], v[24:27], v[12:15]\n\t" \
    "v_mfma_f32_16x16x32_bf16 v[16:19], v[96:99], v[24:27], v[16:19]\n\t" \
    "s_waitcnt vmcnt(7) lgkmcnt(0)\n\t" \
    "ds_read_b64_tr_b16 v[60:61], v220 offset:40960\n\t" \
    "ds_read_b64_tr_b16 v[62:63], v220 offset:43520\n\t" \
    "ds_read_b64_tr_b16 v[64:65], v220 offset:40992\n\t" \
    "ds_read_b64_tr_b16 v[66:67], v220 offset:43552\n\t" \
    "ds_read_b64_tr_b16 v[68:69], v220 offset:41024\n\t" \
    "ds_read_b64_tr_b16 v[70:71], v220 offset:43584\n\t" \
    "ds_read_b64_tr_b16 v[72:73], v220 offset:41056\n\t" \
    "ds_read_b64_tr_b16 v[74:75], v220 offset:43616\n\t" \
    "ds_read_b64_tr_b16 v[76:77], v220 offset:41088\n\t" \
    "ds_read_b64_tr_b16 v[78:79], v220 offset:43648\n\t" \
    "ds_read_b64_tr_b16 v[80:81], v220 offset:46080\n\t" \
    "ds_read_b64_tr_b16 v[82:83], v220 offset:48640\n\t" \
    "ds_read_b64_tr_b16 v[84:85], v220 offset:46112\n\t" \
    "ds_read_b64_tr_b16 v[86:87], v220 offset:48672\n\t" \
    "ds_read_b64_tr_b16 v[88:89], v220 offset:46144\n\t" \
    "ds_read_b64_tr_b16 v[90:91], v220 offset:48704\n\t" \
    "ds_read_b64_tr_b16 v[92:93], v220 offset:46176\n\t" \
    "ds_read_b64_tr_b16 v[94:95], v220 offset:48736\n\t" \
    "ds_read_b64_tr_b16 v[96:97], v220 offset:46208\n\t" \
    "ds_read_b64_tr_b16 v[98:99], v220 offset:48768\n\t" \
    "v_mfma_f32_16x16x32_bf16 v[0:3], v[100:103], v[28:31], v[0:3]\n\t" \
    "v_mfma_f32_16x16x32_bf16 v[4:7], v[104:107], v[28:31], v[4:7]\n\t" \
    "v_mfma_f32_16x16x32_bf16 v[8:11], v[108:111], v[28:31], v[8:11]\n\t" \
    "v_mfma_f32_16x16x32_bf16 v[12:15], v[112:115], v[28:31], v[12:15]\n\t" \
    "v_mfma_f32_16x16x32_bf16 v[16:19], v[116:119], v[28:31], v[16:19]\n\t" \
    "s_waitcnt vmcnt(6)\n\t" \
    "v_mfma_f32_16x16x32_bf16 v[0:3], v[120:123], v[32:35], v[0:3]\n\t" \
    "v_mfma_f32_16x16x32_bf16 v[4:7], v[124:127], v[32:35], v[4:7]\n\t" \
    "v_mfma_f32_16x16x32_bf16 v[8:11], v[128:131], v[32:35], v[8:11]\n\t" \
    "v_mfma_f32_16x16x32_bf16 v[12:15], v[132:135], v[32:35], v[12:15]\n\t" \
    "v_mfma_f32_16x16x32_bf16 v[16:19], v[136:139], v[32:35], v[16:19]\n\t" \
    "s_waitcnt vmcnt(5)\n\t" \
    "v_mfma_f32_16x16x32_bf16 v[0:3], v[140:143], v[36:39], v[0:3]\n\t" \
    "v_mfma_f32_16x16x32_bf16 v[4:7], v[144:147], v[36:39], v[4:7]\n\t" \
    "v_mfma_f32_16x16x32_bf16 v[8:11], v[148:151], v[36:39], v[8:11]\n\t" \
    "v_mfma_f32_16x16x32_bf16 v[12:15], v[152:155], v[36:39], v[12:15]\n\t" \
    "v_mfma_f32_16x16x32_bf16 v[16:19], v[156:159], v[36:39], v[16:19]\n\t" \
    "s_waitcnt vmcnt(4)\n\t" \
    "v_mfma_f32_16x16x32_bf16 v[0:3], v[160:163], v[40:43], v[0:3]\n\t" \
    "v_mfma_f32_16x16x32_bf16 v[4:7], v[164:167], v[40:43], v[4:7]\n\t" \
    "v_mfma_f32_16x16x32_bf16 v[8:11], v[168:171], v[40:43], v[8:11]\n\t" \
    "v_mfma_f32_16x16x32_bf16 v[12:15], v[172:175], v[40:43], v[12:15]\n\t" \
    "v_mfma_f32_16x16x32_bf16 v[16:19], v[176:179], v[40:43], v[16:19]\n\t" \
    "s_waitcnt vmcnt(3)\n\t" \
    "v_mfma_f32_16x16x32_bf16 v[0:3], v[180:183], v[44:47], v[0:3]\n\t" \
    "v_mfma_f32_16x16x32_bf16 v[4:7], v[184:187], v[44:47], v[4:7]\n\t" \
    "v_mfma_f32_16x16x32_bf16 v[8:11], v[188:191], v[44:47], v[8:11]\n\t" \
    "v_mfma_f32_16x16x32_bf16 v[12:15], v[192:195], v[44:47], v[12:15]\n\t" \
    "v_mfma_f32_16x16x32_bf16 v[16:19], v[196:199], v[44:47], v[16:19]\n\t" \
    "s_waitcnt vmcnt(2)\n\t" \
    "v_mfma_f32_16x16x32_bf16 v[0:3], v[200:203], v[48:51], v[0:3]\n\t" \
    "v_mfma_f32_16x16x32_bf16 v[4:7], v[204:207], v[48:51], v[4:7]\n\t" \
    "v_mfma_f32_16x16x32_bf16 v[8:11], v[208:211], v[48:51], v[8:11]\n\t" \
    "v_mfma_f32_16x16x32_bf16 v[12:15], v[212:215], v[48:51], v[12:15]\n\t" \
    "v_mfma_f32_16x16x32_bf16 v[16:19], v[216:219], v[48:51], v[16:19]\n\t" \
    "s_waitcnt vmcnt(1) lgkmcnt(10)\n\t" \
    "v_mfma_f32_16x16x32_bf16 v[0:3], v[60:63], v[52:55], v[0:3]\n\t" \
    "v_mfma_f32_16x16x32_bf16 v[4:7], v[64:67], v[52:55], v[4:7]\n\t" \
    "v_mfma_f32_16x16x32_bf16 v[8:11], v[68:71], v[52:55], v[8:11]\n\t" \
    "v_mfma_f32_16x16x32_bf16 v[12:15], v[72:75], v[52:55], v[12:15]\n\t" \
    "v_mfma_f32_16x16x32_bf16 v[16:19], v[76:79], v[52:55], v[16:19]\n\t" \
    "s_waitcnt vmcnt(0) lgkmcnt(0)\n\t" \
    "v_mfma_f32_16x16x32_bf16 v[0:3], v[80:83], v[56:59], v[0:3]\n\t" \
    "v_mfma_f32_16x16x32_bf16 v[4:7], v[84:87], v[56:59], v[4:7]\n\t" \
    "v_mfma_f32_16x16x32_bf16 v[8:11], v[88:91], v[56:59], v[8:11]\n\t" \
    "v_mfma_f32_16x16x32_bf16 v[12:15], v[92:95], v[56:59], v[12:15]\n\t" \
    "v_mfma_f32_16x16x32_bf16 v[16:19], v[96:99], v[56:59], v[16:19]\n\t" \
    "s_nop 7\n\t"
#define D_MAIN_CLOB "v128","v129","v130","v131","v132","v133","v134","v135","v136","v137","v138","v139","v140","v141","v142","v143","v144","v145","v146","v147","v148","v149","v150","v151","v152","v153","v154","v155","v156","v157","v158","v159","v160","v161","v162","v163","v164","v165","v166","v167","v168","v169","v170","v171","v172","v173","v174","v175","v176","v177","v178","v179","v180","v181","v182","v183","v184","v185","v186","v187","v188","v189","v190","v191","v192","v193","v194","v195","v196","v197","v198","v199","v200","v201","v202","v203","v204","v205","v206","v207"
#define E_MAIN_CLOB "v80","v81","v82","v83","v84","v85","v86","v87","v88","v89","v90","v91","v92","v93","v94","v95","v96","v97","v98","v99","v100","v101","v102","v103","v104","v105","v106","v107","v108","v109","v110","v111","v112","v113","v114","v115","v116","v117","v118","v119","v120","v121","v122","v123","v124","v125","v126","v127","v128","v129","v130","v131","v132","v133","v134","v135","v136","v137","v138","v139","v140","v141","v142","v143","v144","v145","v146","v147","v148","v149","v150","v151","v152","v153","v154","v155","v156","v157","v158","v159","v192","v193","v194","v195","v196","v197","v198","v199","v200","v201","v202","v203","v204","v205","v206","v207","v208","v209","v210","v211","v212","v213","v214","v215","v216","v217","v218","v219","v220","v221","v222","v223"
#define C_MAIN_CLOB "v20","v21","v22","v23","v24","v25","v26","v27","v28","v29","v30","v31","v32","v33","v34","v35","v36","v37","v38","v39","v40","v41","v42","v43","v44","v45","v46","v47","v48","v49","v50","v51","v52","v53","v54","v55","v56","v57","v58","v59","v160","v161","v162","v163","v164","v165","v166","v167","v168","v169","v170","v171","v172","v173","v174","v175","v176","v177","v178","v179","v180","v181","v182","v183","v184","v185","v186","v187","v188","v189","v190","v191","v192","v193","v194","v195","v196","v197","v198","v199","v200","v201","v202","v203","v204","v205","v206","v207","v208","v209","v210","v211","v212","v213","v214","v215","v216","v217","v218","v219"

#define DI __device__ __forceinline__
#define LAS __attribute__((address_space(3)))
#define GAS __attribute__((address_space(1)))
typedef short bf16x8 __attribute__((ext_vector_type(8)));
typedef short bf16x4 __attribute__((ext_vector_type(4)));
typedef float f32x16 __attribute__((ext_vector_type(16)));
typedef float f32x4 __attribute__((ext_vector_type(4)));
typedef float f32x2 __attribute__((ext_vector_type(2)));
typedef unsigned u32x4 __attribute__((ext_vector_type(4)));
typedef unsigned u32x2 __attribute__((ext_vector_type(2)));
typedef unsigned u32x8 __attribute__((ext_vector_type(8)));
typedef unsigned u32x16 __attribute__((ext_vector_type(16)));
typedef __bf16 bf2_t __attribute__((ext_vector_type(2)));

constexpr int SEQ = 2048, DM = 640, FF = 1536;
constexpr float QSCALE = 0.22360679774997896f * 1.4426950408889634f;
constexpr float LN_EPS = 1e-5f;
constexpr float SAFE_T2 = 64.f * 64.f;

constexpr unsigned WS_SYNC = 0;
constexpr unsigned WS_KV = 524288;
constexpr unsigned WS_Q = WS_KV + 256 * 32768;
constexpr unsigned WS_NRM = WS_Q + 256 * 12288;
constexpr unsigned WS_O = WS_NRM + 2 * 256 * 16;
constexpr unsigned WS_H = WS_O + 2621440;
constexpr unsigned WS_U = WS_H + 2621440;
constexpr unsigned WS_ST = WS_U + 1966080;
constexpr unsigned WS_END = WS_ST + 524288;
constexpr unsigned WS_EPOCH = 262144 + 4096;
constexpr unsigned FSTR = 256;
constexpr unsigned FLAG_KV = 0;
constexpr unsigned FLAG_O = 65536, FLAG_H = 131072, FLAG_U = 196608;

constexpr unsigned L_SLOT = 32768;
constexpr unsigned L_WO = 0;
constexpr unsigned L_KV0 = 102400, L_KVS = 24576;
constexpr unsigned L_PIECE = 65536;
constexpr unsigned L_OST = 102400, L_OX = 126976;
constexpr unsigned L_CX = 131072, L_HT = 131072, L_ST = 155648, L_MU = 156672;
constexpr unsigned L_SCR = 151552;
constexpr unsigned L_PCS = L_SCR;
constexpr unsigned L_PSS = L_SCR + 2048;
constexpr unsigned L_SUMX = L_SCR + 3072;
constexpr unsigned L_CSW = L_SCR + 6144;
constexpr unsigned L_DUMP = 163584;
constexpr unsigned LDS_BYTES = 163840;
constexpr int POLL_DELAY = 10;

#define SBAR() __builtin_amdgcn_sched_barrier(0)
#define PIN(x) asm volatile("" : "+v"(x))
#define SPIN(x) asm volatile("" : "+s"(x))
#define WAIT_BAR(N) asm volatile("s_waitcnt vmcnt(" #N ") lgkmcnt(0)\n\ts_barrier" ::: "memory")
#define LBAR() asm volatile("s_waitcnt lgkmcnt(0)\n\ts_barrier" ::: "memory")
#define MFMA32(a, b, c) __builtin_amdgcn_mfma_f32_32x32x16_bf16(a, b, c, 0, 0, 0)
#define MFMA16(a, b, c) __builtin_amdgcn_mfma_f32_16x16x32_bf16(a, b, c, 0, 0, 0)
#define MFMA32K8(a, b, c) __builtin_amdgcn_mfma_f32_32x32x8bf16_1k(a, b, c, 0, 0, 0)

DI unsigned cvtpk(float lo, float hi) { f32x2 v = {lo, hi}; bf2_t b = __builtin_convertvector(v, bf2_t); return __builtin_bit_cast(unsigned, b); }
DI float bflo(unsigned x) { return __uint_as_float(x << 16); }
DI float bfhi(unsigned x) { return __uint_as_float(x & 0xffff0000u); }
DI void glds16(unsigned voff, const void* sbase, unsigned lds_addr) {
    unsigned sv; lds_addr = __builtin_amdgcn_readfirstlane(lds_addr);
    asm volatile("s_mov_b32 %0, m0\n\ts_mov_b32 m0, %3\n\ts_nop 0\n\tglobal_load_lds_dwordx4 %1, %2\n\ts_mov_b32 m0, %0"
                 : "=&s"(sv) : "v"(voff), "s"(sbase), "s"(lds_addr) : "memory");
}
DI void glds16_sc1(unsigned voff, const void* sbase, unsigned lds_addr) {
    unsigned sv; lds_addr = __builtin_amdgcn_readfirstlane(lds_addr);
    asm volatile("s_mov_b32 %0, m0\n\ts_mov_b32 m0, %3\n\ts_nop 0\n\tglobal_load_lds_dwordx4 %1, %2 sc1\n\ts_mov_b32 m0, %0"
                 : "=&s"(sv) : "v"(voff), "s"(sbase), "s"(lds_addr) : "memory");
}
DI void l2_touch(unsigned voff, __amdgpu_buffer_rsrc_t rs, unsigned lds_dump) {
    unsigned sv;
    asm volatile("s_mov_b32 %0, m0\n\ts_mov_b32 m0, %3\n\ts_nop 0\n\tbuffer_load_dword %1, %2, 0 offen lds\n\ts_mov_b32 m0, %0"
                 : "=&s"(sv) : "v"(voff), "s"(rs), "s"(lds_dump) : "memory");
}
DI void touch64(const char* a, unsigned lds_dump) {
    unsigned sv;
    asm volatile("s_mov_b32 %0, m0\n\ts_mov_b32 m0, %2\n\ts_nop 0\n\tglobal_load_lds_dword %1, off\n\ts_mov_b32 m0, %0"
                 : "=&s"(sv) : "v"(a), "s"(lds_dump) : "memory");
}
template <typename T> DI T lds_ld(LAS char* lds, unsigned off) { return *(LAS const T*)(lds + off); }
template <typename T> DI void lds_st(LAS char* lds, unsigned off, T v) { *(LAS T*)(lds + off) = v; }
DI bf16x4 lds_tr(LAS char* lds, unsigned off) { return __builtin_amdgcn_ds_read_tr16_b64_v4i16((LAS bf16x4*)(lds + off)); }
DI bf16x8 cat44(bf16x4 a, bf16x4 b) { return __builtin_shufflevector(a, b, 0, 1, 2, 3, 4, 5, 6, 7); }
DI float half_sum(float v) { auto rr = __builtin_amdgcn_permlane32_swap(__float_as_uint(v), __float_as_uint(v), false, false); return __uint_as_float(rr[0]) + __uint_as_float(rr[1]); }
DI float half_max(float v) { auto rr = __builtin_amdgcn_permlane32_swap(__float_as_uint(v), __float_as_uint(v), false, false); return fmaxf(__uint_as_float(rr[0]), __uint_as_float(rr[1])); }
DI float hi_half(float v) { auto rr = __builtin_amdgcn_permlane32_swap(__float_as_uint(v), __float_as_uint(v), false, false); return __uint_as_float(rr[1]); }
template <int CTRL> DI float dpp_f(float v) { return __int_as_float(__builtin_amdgcn_update_dpp(__float_as_int(v), __float_as_int(v), CTRL, 0xf, 0xf, false)); }
template <int CTRL> DI unsigned dpp_u(unsigned v) { return (unsigned)__builtin_amdgcn_update_dpp((int)v, (int)v, CTRL, 0xf, 0xf, false); }
DI float quad_max(float f) { unsigned v = __float_as_uint(f); v = max(v, dpp_u<0xB1>(v)); v = max(v, dpp_u<0x4E>(v)); return __uint_as_float(v); }
DI float wave_max(float f) {
    unsigned v = __float_as_uint(quad_max(f));
    v = max(v, dpp_u<0x141>(v));
    v = max(v, dpp_u<0x140>(v));
    { auto rr = __builtin_amdgcn_permlane16_swap(v, v, false, false); v = max(rr[0], rr[1]); }
    { auto rr = __builtin_amdgcn_permlane32_swap(v, v, false, false); v = max(rr[0], rr[1]); }
    return __uint_as_float(v);
}

typedef GAS unsigned long long gu64;
DI void set_flag(char* a, unsigned long long tag) { __hip_atomic_store((gu64*)a, tag, __ATOMIC_RELAXED, __HIP_MEMORY_SCOPE_AGENT); }
DI unsigned long long ld_flag(const char* a) { return __hip_atomic_load((gu64*)a, __ATOMIC_RELAXED, __HIP_MEMORY_SCOPE_AGENT); }
DI void wait_flags(const char* a, unsigned long long tag) {
    for (unsigned spins = 0; spins < (1u << 20); spins++) {
        const unsigned long long g = __hip_atomic_load((gu64*)a, __ATOMIC_RELAXED, __HIP_MEMORY_SCOPE_AGENT);
        if (__all(g == tag)) break;
    }
    asm volatile("" ::: "memory");
}
struct Params {
    const char* x; const char* wqkv; const char* wo; const char* w1; const char* b1; const char* w2; const char* b2;
    char* out; char* ws;
};

constexpr int TLB_STEP = 1048576, TLB_NW = (WS_END + TLB_STEP - 1) / TLB_STEP, TLB_N1 = (SEQ * 3072 + TLB_STEP - 1) / TLB_STEP, TLB_NO = (SEQ * 1280 + TLB_STEP - 1) / TLB_STEP;
DI void tlb_touch(const Params& p, int i, unsigned dump) {
    const char* a = i < TLB_NW ? p.ws + (size_t)i * TLB_STEP : (i < TLB_NW + TLB_N1 ? p.w1 + (size_t)(i - TLB_NW) * TLB_STEP : (i < TLB_NW + 2 * TLB_N1 ? p.w2 + (size_t)(i - TLB_NW - TLB_N1) * TLB_STEP :
                    (i < TLB_NW + 2 * TLB_N1 + TLB_NO ? p.out + (size_t)(i - TLB_NW - 2 * TLB_N1) * TLB_STEP : (i == TLB_NW + 2 * TLB_N1 + TLB_NO ? p.wo : (i == TLB_NW + 2 * TLB_N1 + TLB_NO + 1 ? p.b1 : p.b2)))));
    if (i < TLB_NW + 2 * TLB_N1 + TLB_NO + 3) touch64(a, dump);
}
#define TLB_TOUCH() do { if (w == 7) tlb_touch(p, lane, lds0 + L_DUMP); } while (0)

__global__ __launch_bounds__(512, 2) void mega(Params p) {
    extern __shared__ __attribute__((aligned(16))) char lds_raw[];
    LAS char* lds = (LAS char*)lds_raw;
    const unsigned lds0 = (unsigned)(size_t)lds_raw;
    const int tid = threadIdx.x, lane = tid & 63;
    const int w = __builtin_amdgcn_readfirstlane(tid >> 6);
    const int r = lane & 31, h = lane >> 5;
    const int b = blockIdx.x, xcd = b & 7, jb = b >> 3;
    const int hp = xcd * 2 + (jb & 1), t = jb >> 1;
    const int cg = (xcd & 1) * 4 + (jb & 3), rt = (xcd >> 1) * 8 + (jb >> 2);
    const int ta = (xcd & 1) * 8 + (jb & 7), ct = (xcd >> 1) * 4 + (jb >> 3);
    const __amdgpu_buffer_rsrc_t wsr = __builtin_amdgcn_make_buffer_rsrc(p.ws, 0, WS_END, 0x00020000);

    const bool hasA = ct < 15;
    const int tt = w & 3, nh = w >> 2;
    const int ty = ct / 5, lc0 = 128 * (ct - 5 * ty), p0 = lc0 / 40;
    const __amdgpu_buffer_rsrc_t rsx = __builtin_amdgcn_make_buffer_rsrc((void*)(p.x + (size_t)ta * 163840), 0, 163840, 0x00020000);
    const __amdgpu_buffer_rsrc_t rsw = __builtin_amdgcn_make_buffer_rsrc((void*)p.wqkv, 0, 640 * 3840, 0x00020000);
    const __amdgpu_buffer_rsrc_t rxx = __builtin_amdgcn_make_buffer_rsrc((void*)(p.x + (size_t)(xcd & 1) * 1024 * 1280), 0, 1024 * 1280, 0x00020000);
    const unsigned pfx = (jb * 32 + (lane & 31)) * 1280 + (lane >> 5) * 128;
    unsigned long long epoch;
    if (hasA) {
        u32x2 dx, dw;
#pragma unroll
        for (int ii = 0; ii < 2; ii++) {
            const int i = 2 * w + ii;
            const int row = 8 * i + (lane >> 3), c1 = lane & 7;
            dx[ii] = row * 1280 + ((c1 ^ ((row >> 1) & 7)) << 4) - ii * 1024;
            const int k = 4 * i + (lane >> 4), c = (lane & 15) ^ ((k & 3) << 2);
            dw[ii] = k * 3840 + ct * 256 + c * 16 - ii * 1024;
        }
        const unsigned mx = lds0 + w * 2048;
#define QKV_PRO(N) do { unsigned sv, sx, sw; asm volatile(QKV_PRO##N##_ASM : [sv] "=&s"(sv), [sx] "=&s"(sx), [sw] "=&s"(sw) \
                     : "{v[184:185]}"(dx), "{v[186:187]}"(dw), [rx] "s"(rsx), [rw] "s"(rsw), [mx] "s"(mx) : "memory", "scc"); } while (0)
        QKV_PRO(0);
        const int rot = tt ^ (nh << 1);
        u32x4 xa0, xa1; u32x8 wa0, wa1;
        {
            const int m = (r >> 1) & 7;
            const int q = (lane & 15) >> 2, pp = lane & 3, g = (lane >> 4) & 1;
#pragma unroll
            for (int s = 0; s < 4; s++) {
                xa0[s] = lds0 + (32 * tt + r) * 128 + (((h ^ m) << 4) ^ ((s ^ rot) << 5));
                xa1[s] = xa0[s] + 65536;
#pragma unroll
                for (int u = 0; u < 2; u++) {
                    wa0[4 * u + s] = lds0 + 16384 + (8 * h + q) * 256 + (((2 * nh + u) ^ q) << 6) + g * 32 + pp * 8 + ((s ^ rot) << 12);
                    wa1[4 * u + s] = wa0[4 * u + s] + 65536;
                }
            }
        }
        PIN(xa0); PIN(xa1); PIN(wa0); PIN(wa1);
        QKV_PRO(1);
        const int tok = 32 * tt + r;
        unsigned dst[8];
        {
            const int kq = r & 15;
            const unsigned vpos = tt * 1408 + (r >> 4) * 704 + ((kq >> 2) & 1) * 352 + ((kq >> 3) * 4 + (kq & 3)) * 2;
#pragma unroll
            for (int g = 0; g < 8; g++) {
                const int lc = lc0 + 64 * nh + 8 * g + 4 * h;
                const int hd = ((lc * 3277) >> 16) - 2 * p0, d0 = lc - 20 * (hd + 2 * p0);
                dst[g] = L_PIECE + hd * 6144 + (ty == 2 ? vpos + d0 * 16 : (d0 < 16 ? (d0 >> 3) * 2048 + tok * 16 + (d0 & 4) * 2 : 4096 + tok * 8));
            }
        }
#pragma unroll
        for (int g = 0; g < 8; g++) PIN(dst[g]);
        QKV_PRO(2);
        unsigned pfw;
        {
            const int nl = (xcd >> 1) == 3 ? 6 : 8, sub = lane >> 4, n = jb * 16 + (lane & 15), row = n / nl;
            pfw = lane < 48 ? (sub * 64 + row) * 3840 + (xcd >> 1) * 1024 + (n - row * nl) * 128 : 0x7ffffff0u;
        }
        {
            const unsigned dump = lds0 + L_DUMP;
            if (w == 0) l2_touch(pfx + 3 * 128, rxx, dump);
            if (w == 1) l2_touch(lane < 48 ? pfw + 3 * 245760 : pfw, rsw, dump);
            if (w == 4 || w == 5) {
                const __amdgpu_buffer_rsrc_t ro = __builtin_amdgcn_make_buffer_rsrc((void*)p.wo, 0, DM * 1280, 0x00020000);
                const int i = (w - 4) * 64 + lane, n = jb * 100 + i, row = n / 5;
                l2_touch(i < 100 ? row * 1280 + (xcd & 1) * 640 + (n - row * 5) * 128 : 0x7ffffff0u, ro, dump);
            }
        }
        TLB_TOUCH();
        const int hc = 40 * p0 + 20 * w;
        const int ulo = __builtin_amdgcn_readfirstlane(min(max((lc0 - hc) >> 2, 0), 5)), uhi = __builtin_amdgcn_readfirstlane(min(max((lc0 + 128 - hc) >> 2, 0), 5));
        const bool padown = uhi == 5 && ulo <= 4;
        unsigned a_pss = L_PSS + (nh * 128 + tok) * 4, a_psx = L_SUMX + (nh * 128 + tok) * 4, a_pcs = L_PCS + (tt * 128 + 64 * nh + r) * 4, a_tok = tok * 4;
        unsigned a_pc = L_PCS + (64 * nh + lane) * 4, a_csw = L_CSW + w * 256 + lane * 4, a_csr = L_CSW + w * 256 + 16 * h;
        unsigned a_hl = L_PIECE + w * 6144 + lane * 16;
        unsigned a_hg = (ty == 0 ? WS_Q + ((p0 + (w >> 1)) * 16 + ta) * 12288 : WS_KV + (p0 + (w >> 1)) * 524288 + ta * 32768 + (ty - 1) * 10240) + (w & 1) * (ty == 2 ? 5632 : 5120) + lane * 16;
        unsigned vmask = 0;
        if (ty == 2) {
#pragma unroll
            for (int ps = 0; ps < 6; ps++) {
                const int idx = ps * 64 + lane, blk = (idx * 2979) >> 16, d = idx - 22 * blk;
                const bool o = idx < 352 && (d < 20 ? ((d >> 2) >= ulo && (d >> 2) < uhi) : padown);
                vmask |= (o ? 1u : 0u) << ps;
            }
        }
        PIN(a_pss); PIN(a_psx); PIN(a_pcs); PIN(a_tok); PIN(a_pc); PIN(a_csw); PIN(a_csr); PIN(a_hl); PIN(a_hg); PIN(vmask);
        f32x16 acc0, acc1, ss16; f32x4 cs4, sxa, sxb;
        {
            unsigned sv, sx, sw; unsigned long long tsq = 0;
            asm volatile(QKV_ASM : "={v[0:15]}"(acc0), "={v[16:31]}"(acc1), "={v[32:47]}"(ss16), "={v[48:51]}"(cs4), "={v[58:61]}"(sxa), "={v[190:193]}"(sxb),
                           [sv] "=&s"(sv), [sx] "=&s"(sx), [sw] "=&s"(sw)
                         : "{v[160:163]}"(xa0), "{v[164:167]}"(xa1), "{v[168:175]}"(wa0), "{v[176:183]}"(wa1), "{v[184:185]}"(dx), "{v[186:187]}"(dw),
                           "{v188}"(pfx), "{v189}"(pfw), [rxx] "s"(rxx), [wid] "s"(w),
                           [rx] "s"(rsx), [rw] "s"(rsw), [mx] "s"(mx)
                         : QKV_CLOB, "memory", "scc");
        }
        epoch = __hip_atomic_load((gu64*)(p.ws + WS_EPOCH), __ATOMIC_RELAXED, __HIP_MEMORY_SCOPE_AGENT);
        {
            const f32x4 t4 = (f32x4){ss16[0], ss16[1], ss16[2], ss16[3]} + (f32x4){ss16[4], ss16[5], ss16[6], ss16[7]} + (f32x4){ss16[8], ss16[9], ss16[10], ss16[11]} + (f32x4){ss16[12], ss16[13], ss16[14], ss16[15]};
            const f32x4 x4 = sxa + sxb;
            const float lo2 = (lane & 1) ? t4[1] : t4[0], hi2 = (lane & 1) ? t4[3] : t4[2];
            const float lo1 = (lane & 1) ? x4[1] : x4[0], hi1 = (lane & 1) ? x4[3] : x4[2];
            const float ss = half_sum((lane & 2) ? hi2 : lo2), sx = half_sum((lane & 2) ? hi1 : lo1);
            const float c0 = half_sum(cs4[0] + cs4[1]), c1 = half_sum(cs4[2] + cs4[3]);
            if (h == 0) {
                lds_st<float>(lds, a_pss, ss);
                lds_st<float>(lds, a_psx, sx);
                lds_st<float>(lds, a_pcs, c0);
                lds_st<float>(lds, a_pcs + 128, c1);
            }
        }
        LBAR();
        {
            const float p0_ = lds_ld<float>(lds, a_pc), p1 = lds_ld<float>(lds, a_pc + 512), p2 = lds_ld<float>(lds, a_pc + 1024), p3 = lds_ld<float>(lds, a_pc + 1536);
            const float x0 = lds_ld<float>(lds, L_SUMX + a_tok), x1 = lds_ld<float>(lds, L_SUMX + 512 + a_tok), q0 = lds_ld<float>(lds, L_PSS + a_tok), q1 = lds_ld<float>(lds, L_PSS + 512 + a_tok);
            lds_st<float>(lds, a_csw, (p0_ + p1) + (p2 + p3));
            f32x4 cs[8];
#pragma unroll
            for (int g = 0; g < 8; g++) cs[g] = lds_ld<f32x4>(lds, a_csr + g * 32);
            if (ty == 2 && padown && lane < 32) {
                const int blk = lane >> 1; const unsigned v = (lane & 1) ? 0x3F803F80u : 0u;
                lds_st<u32x4>(lds, L_PIECE + w * 6144 + blk * 352 + ((lane & 1) ? 20 : 21) * 16, (u32x4){v, v, v, v});
            }
            const float mean = (x0 + x1) * (1.f / DM);
            const float var = fmaxf((q0 + q1) * (1.f / DM) - mean * mean, 0.f);
            const float rstd = __builtin_amdgcn_rsqf(var + LN_EPS), sc = ty == 0 ? rstd * QSCALE : rstd, nm = -mean;
            unsigned pk[8][2];
            auto norm = [&](int g, float a0, float a1, float a2, float a3) {
                pk[g][0] = cvtpk(fmaf(nm, cs[g][0], a0) * sc, fmaf(nm, cs[g][1], a1) * sc); pk[g][1] = cvtpk(fmaf(nm, cs[g][2], a2) * sc, fmaf(nm, cs[g][3], a3) * sc);
            };
#pragma unroll
            for (int g = 0; g < 4; g++) { norm(g, acc0[4 * g], acc0[4 * g + 1], acc0[4 * g + 2], acc0[4 * g + 3]); norm(4 + g, acc1[4 * g], acc1[4 * g + 1], acc1[4 * g + 2], acc1[4 * g + 3]); }
            if (ty != 2) {
#pragma unroll
                for (int g = 0; g < 8; g++) lds_st<u32x2>(lds, dst[g], (u32x2){pk[g][0], pk[g][1]});
            } else {
#pragma unroll
                for (int g = 0; g < 8; g++) {
                    lds_st<short>(lds, dst[g], (short)pk[g][0]); lds_st<short>(lds, dst[g] + 16, (short)(pk[g][0] >> 16));
                    lds_st<short>(lds, dst[g] + 32, (short)pk[g][1]); lds_st<short>(lds, dst[g] + 48, (short)(pk[g][1] >> 16));
                }
            }
        }
        LBAR();
        if (ty != 2) {
            u32x4 c[2][2];
#pragma unroll
            for (int pl = 0; pl < 2; pl++) { c[pl][0] = lds_ld<u32x4>(lds, a_hl + pl * 2048); c[pl][1] = lds_ld<u32x4>(lds, a_hl + pl * 2048 + 1024); }
            const u32x4 c2 = lds_ld<u32x4>(lds, a_hl + 4096);
            const u32x2 e0 = lds_ld<u32x2>(lds, a_hl + 4096 - lane * 8), e1 = lds_ld<u32x2>(lds, a_hl + 4096 + 512 - lane * 8);
            float n2[2] = {0.f, 0.f};
            auto sq = [](unsigned a, unsigned c_) { return fmaf(bflo(a), bflo(a), bfhi(a) * bfhi(a)) + fmaf(bflo(c_), bflo(c_), bfhi(c_) * bfhi(c_)); };
#pragma unroll
            for (int pl = 0; pl < 2; pl++) {
                const bool lo = 2 * pl >= ulo && 2 * pl < uhi, hi = 2 * pl + 1 >= ulo && 2 * pl + 1 < uhi;
#pragma unroll
                for (int i = 0; i < 2; i++) {
                    const unsigned ga = a_hg + pl * 2048 + i * 1024;
                    if (lo && hi) __builtin_amdgcn_raw_buffer_store_b128(c[pl][i], wsr, ga, 0, 16);
                    else if (lo) __builtin_amdgcn_raw_buffer_store_b64((u32x2){c[pl][i][0], c[pl][i][1]}, wsr, ga, 0, 16);
                    else if (hi) __builtin_amdgcn_raw_buffer_store_b64((u32x2){c[pl][i][2], c[pl][i][3]}, wsr, ga + 8, 0, 16);
                    if (lo) n2[i] += sq(c[pl][i][0], c[pl][i][1]);
                    if (hi) n2[i] += sq(c[pl][i][2], c[pl][i][3]);
                }
            }
            if (padown) {
                __builtin_amdgcn_raw_buffer_store_b128(c2, wsr, a_hg + 4096, 0, 16);
                n2[0] += sq(e0[0], e0[1]); n2[1] += sq(e1[0], e1[1]);
            }
            const float m0 = (n2[0] <= 1.0e38f) ? n2[0] : 1.0e38f, m1 = (n2[1] <= 1.0e38f) ? n2[1] : 1.0e38f;
            const float nm_ = wave_max(m0 > m1 ? m0 : m1);
            if (lane == 0) __builtin_amdgcn_raw_buffer_store_b32(__float_as_uint(nm_), wsr, WS_NRM + (((ty * 5 + ct - 5 * ty) * 16 + ta) * 8 + w) * 4, 0, 16);
        } else {
            u32x4 c[6];
#pragma unroll
            for (int ps = 0; ps < 6; ps++) c[ps] = lds_ld<u32x4>(lds, a_hl + ps * 1024);
#pragma unroll
            for (int ps = 0; ps < 6; ps++) if ((vmask >> ps) & 1) __builtin_amdgcn_raw_buffer_store_b128(c[ps], wsr, a_hg + ps * 1024, 0, 16);
        }
    } else {
        TLB_TOUCH();
        const unsigned dump = lds0 + L_DUMP;
        if (w < 4) l2_touch(pfx + (3 + 2 * w) * 128, rxx, dump);
        if (w == 4 || w == 5) {
            const __amdgpu_buffer_rsrc_t ro = __builtin_amdgcn_make_buffer_rsrc((void*)p.wo, 0, DM * 1280, 0x00020000);
            const int i = (w - 4) * 64 + lane, n = jb * 100 + i, row = n / 5;
            l2_touch(i < 100 ? row * 1280 + (xcd & 1) * 640 + (n - row * 5) * 128 : 0x7ffffff0u, ro, dump);
        }
        epoch = __hip_atomic_load((gu64*)(p.ws + WS_EPOCH), __ATOMIC_RELAXED, __HIP_MEMORY_SCOPE_AGENT);
    }
    unsigned long long tag64;
    {
        unsigned long long z = ((unsigned long long)__builtin_amdgcn_readfirstlane((unsigned)(epoch >> 32)) << 32) | (unsigned)__builtin_amdgcn_readfirstlane((unsigned)epoch);
        z += 0x9E3779B97F4A7C15ull; z = (z ^ (z >> 30)) * 0xBF58476D1CE4E5B9ull; z = (z ^ (z >> 27)) * 0x94D049BB133111EBull; tag64 = z ^ (z >> 31);
    }
    WAIT_BAR(0);
    if (hasA && tid == 0) set_flag(p.ws + WS_SYNC + FLAG_KV + (ct * 16 + ta) * FSTR, tag64);
    if (w > 0) {
        unsigned pat[5];
#pragma unroll
        for (int m5 = 0; m5 < 5; m5++) {
            const int n = m5 * 64 + lane, pos = (n * 205) >> 11, c = n - pos * 10;
            const int crow = 8 * ((pos >> 2) & 3) + 4 * (pos >> 4) + (pos & 3);
            pat[m5] = crow * 1280 + cg * 160 + c * 16;
        }
        for (int B = w - 1; B < 20; B += 7) {
#pragma unroll
            for (int m5 = 0; m5 < 5; m5++) glds16(pat[m5], p.wo + (size_t)B * 40960, lds0 + L_WO + B * 5120 + m5 * 1024);
        }
    }
    const int hh = w >> 2, kh = (w >> 1) & 1, qg = w & 1;
    unsigned ka = hh * 5120 + h * 2048 + (64 * kh + r) * 16;
    unsigned kb = hh * 5120 + 4096 + (64 * kh + r) * 8;
    unsigned va = 10240 + hh * 5632 + kh * 2816 + (h * 22 + (r <= 20 ? r : 21)) * 16;
    unsigned kvown = WS_KV + hp * 524288 + t * 32768;
    unsigned qa = WS_Q + (hp * 16 + t) * 12288 + hh * 5120 + (64 * qg + r) * 16;
    const unsigned qb = h ? 0x7ffffff0u : WS_Q + (hp * 16 + t) * 12288 + hh * 5120 + 4096 + (64 * qg + r) * 8;
    unsigned kna, qna;
    {
        const int hd = (lane >> 1) & 1, c0 = 40 * hp + 20 * hd, cA = c0 >> 7, cB = (c0 + 19) >> 7, cc = (lane & 1) ? cB : cA;
        const int slot = 2 * (hp - (128 * cc) / 40) + hd;
        const bool used = (lane & 1) == 0 || cB != cA;
        kna = used ? WS_NRM + (((5 + cc) * 16 + (lane >> 2)) * 8 + slot) * 4 : 0x7ffffff0u;
        qna = used ? WS_NRM + ((cc * 16 + t) * 8 + slot) * 4 : 0x7ffffff0u;
    }
    unsigned oxw = L_OX + w * 3072 + lane * 16, oxr = L_OX + (w ^ 2) * 3072 + lane * 16;
    unsigned ostw = L_OST + (64 * qg + 32 * kh + r) * 80 + hh * 40 + h * 8;
    unsigned ocl0, ocg0, ocl1, ocg1;
    {
        auto cell = [&](int idx, unsigned& l, unsigned& g) {
            const int s16_ = idx & 15, q5 = idx >> 4, rt8 = (q5 * 205) >> 10, cc = q5 - rt8 * 5, cgl = 5 * hp + cc;
            l = L_OST + (rt8 * 16 + s16_) * 80 + cc * 16;
            g = WS_O + (((t * 8 + rt8) * 20 + (cgl >> 2)) * 64 + (cgl & 3) * 16 + s16_) * 16;
        };
        cell(tid, ocl0, ocg0); cell(512 + (tid & 127), ocl1, ocg1);
    }
    const __amdgpu_buffer_rsrc_t r1 = __builtin_amdgcn_make_buffer_rsrc((void*)p.w1, 0, SEQ * 3072, 0x00020000);
    const __amdgpu_buffer_rsrc_t r2 = __builtin_amdgcn_make_buffer_rsrc((void*)p.w2, 0, FF * 4096, 0x00020000);
    const __amdgpu_buffer_rsrc_t rp1 = w < 6 ? r1 : r2;
    unsigned vp1, vp2;
    {
        const unsigned a1 = (jb * 64 + lane) * 3072 + (xcd >> 1) * 768, a2 = (jb * 48 + (lane >> 3)) * 4096 + (xcd >> 1) * 1024 + (lane & 7) * 128;
        vp1 = w < 6 ? a1 + w * 128 : a2 + (w - 6) * 32768; vp2 = a2 + (w + 2) * 32768;
    }
    unsigned jp1 = __builtin_amdgcn_readfirstlane((w + 2) << 15), jp2 = __builtin_amdgcn_readfirstlane(w < 4 ? (w + 10) << 15 : 0xffffffffu);
    unsigned ml = __builtin_amdgcn_readfirstlane(lds0 + L_KV0 + w * 3072);
    unsigned a164 = lds0 + L_KV0 + ka, a165 = lds0 + L_KV0 + kb, a166 = lds0 + L_KV0 + va, a167 = w < 7 ? w * 3072 + lane * 16 : 0x7ffffff0u, a168 = qa + 2048 * h, a169 = qb;
    PIN(a164); PIN(a165); PIN(a166); PIN(a167); PIN(a168); PIN(a169); PIN(vp1); PIN(vp2); PIN(kna); PIN(qna);
    PIN(oxw); PIN(oxr); PIN(ostw); PIN(ocl0); PIN(ocg0); PIN(ocl1); PIN(ocg1);
    kvown = __builtin_amdgcn_readfirstlane(kvown);
    SPIN(ml); SPIN(jp1); SPIN(jp2); SPIN(kvown);
    if (w == 0) {
        const int cA = (40 * hp) >> 7, cB = (40 * hp + 39) >> 7, cc = (lane & 1) ? cB : cA;
        const char* f1 = p.ws + WS_SYNC + FLAG_KV + (((lane < 32 ? 5 : 10) + cc) * 16 + ((lane >> 1) & 15)) * FSTR;
        const char* f2 = p.ws + WS_SYNC + FLAG_KV + (cc * 16 + t) * FSTR;
        for (unsigned spins = 0; spins < (1u << 20); spins++) {
            const unsigned long long g1 = ld_flag(f1), g2 = ld_flag(f2);
            if (__all(g1 == tag64 && g2 == tag64)) break;
        }
        asm volatile("" ::: "memory");
    }
    LBAR();
    auto stageKV = [&](int j) {
        const char* src = p.ws + WS_KV + (size_t)hp * 524288 + (size_t)((t ^ j) & 15) * 32768;
#pragma unroll
        for (int i = 0; i < 3; i++) if (w < 7) glds16_sc1((w * 3 + i) * 1024 + lane * 16, src, lds0 + L_KV0 + (j & 1) * L_KVS + (w * 3 + i) * 1024);
    };
    const float kn = __uint_as_float(__builtin_amdgcn_raw_buffer_load_b32(wsr, kna, 0, 16));
    const float qn = __uint_as_float(__builtin_amdgcn_raw_buffer_load_b32(wsr, qna, 0, 16));
    f32x16 OA, OB;
    {
        unsigned sJ, sv, sso; unsigned long long tsa = 0;
        asm volatile(ATTN_ASM
            : "={v[0:15]}"(OA), "={v[16:31]}"(OB), [J] "=&s"(sJ), [sv] "=&s"(sv), [so] "=&s"(sso)
            : "{v164}"(a164), "{v165}"(a165), "{v166}"(a166), "{v167}"(a167), "{v168}"(a168), "{v169}"(a169), "{v170}"(vp1), "{v171}"(vp2),
              [rs] "s"(wsr), [ml] "s"(ml), [T0] "s"(kvown), [rp1] "s"(rp1), [rp2] "s"(r2), [JP1] "s"(jp1), [JP2] "s"(jp2)
            : ATTN_CLOB, "memory", "scc");
    }
    bool unsafe = !(wave_max(kn + dpp_f<0xB1>(kn)) * quad_max(qn + dpp_f<0xB1>(qn)) <= SAFE_T2);
    if (__builtin_amdgcn_readfirstlane(unsafe)) {
        const u32x4 x1 = __builtin_amdgcn_raw_buffer_load_b128(wsr, qa + 2048 * h, 0, 16), x2 = __builtin_amdgcn_raw_buffer_load_b128(wsr, qa + 512 + 2048 * h, 0, 16);
        const u32x2 y1 = __builtin_amdgcn_raw_buffer_load_b64(wsr, qb, 0, 16), y2 = __builtin_amdgcn_raw_buffer_load_b64(wsr, qb + 256, 0, 16);
        const bf16x8 q1a = __builtin_bit_cast(bf16x8, x1), q1b = __builtin_bit_cast(bf16x8, x2);
        const bf16x4 q2a = __builtin_bit_cast(bf16x4, y1), q2b = __builtin_bit_cast(bf16x4, y2);
#define EX(x) __builtin_amdgcn_exp2f(x)
        float mA = -INFINITY, mB = -INFINITY; OA = f32x16{}; OB = f32x16{};
        auto tile = [&](f32x16& O, float& m, const bf16x8& q1, const bf16x4& q2, const bf16x8& k1, const bf16x4& k2, const bf16x8& v0, const bf16x8& v1) {
            f32x16 S = MFMA32(k1, q1, f32x16{}); S = MFMA32K8(k2, q2, S);
            float mx = S[0];
#pragma unroll
            for (int e = 1; e < 16; e++) mx = fmaxf(mx, S[e]);
            mx = half_max(mx);
            const float mn = fmaxf(m, mx);
            const float ms = (mn == -INFINITY) ? 0.f : mn;
            const float alpha = EX(m - ms);
            m = mn;
#pragma unroll
            for (int e = 0; e < 16; e++) O[e] *= alpha;
            u32x4 P0, P1;
#pragma unroll
            for (int e = 0; e < 4; e++) { P0[e] = cvtpk(EX(S[2 * e] - ms), EX(S[2 * e + 1] - ms)); P1[e] = cvtpk(EX(S[8 + 2 * e] - ms), EX(S[9 + 2 * e] - ms)); }
            O = MFMA32(v0, __builtin_bit_cast(bf16x8, P0), O);
            O = MFMA32(v1, __builtin_bit_cast(bf16x8, P1), O);
        };
        for (int j = 0; j < 16; j++) {
            LBAR(); stageKV(j);
            WAIT_BAR(0);
            const unsigned so = L_KV0 + (j & 1) * L_KVS;
            for (int i = 0; i < 2; i++) {
                const bf16x8 k1 = lds_ld<bf16x8>(lds, ka + so + i * 512); const bf16x4 k2 = lds_ld<bf16x4>(lds, kb + so + i * 256);
                const bf16x8 v0 = lds_ld<bf16x8>(lds, va + so + i * 1408), v1 = lds_ld<bf16x8>(lds, va + so + i * 1408 + 704);
                tile(OA, mA, q1a, q2a, k1, k2, v0, v1);
                tile(OB, mB, q1b, q2b, k1, k2, v0, v1);
            }
        }
        LBAR();
        if (h == 0) { lds_st<float>(lds, L_PCS + (w * 64 + r) * 4, mA); lds_st<float>(lds, L_PCS + (w * 64 + 32 + r) * 4, mB); }
        LBAR();
        const float oA = lds_ld<float>(lds, L_PCS + ((w ^ 2) * 64 + r) * 4), oB = lds_ld<float>(lds, L_PCS + ((w ^ 2) * 64 + 32 + r) * 4);
        const float fA = EX(mA - fmaxf(mA, oA)), fB = EX(mB - fmaxf(mB, oB));
#pragma unroll
        for (int e = 0; e < 16; e++) { OA[e] *= fA; OB[e] *= fB; }
        LBAR();
    }
    {
        auto send = [&](const f32x16& v) {
#pragma unroll
            for (int i = 0; i < 3; i++) lds_st<f32x4>(lds, oxw + i * 1024, (f32x4){v[4 * i], v[4 * i + 1], v[4 * i + 2], v[4 * i + 3]});
        };
        auto finish = [&](const f32x16& v) {
            const f32x4 o0 = lds_ld<f32x4>(lds, oxr), o1 = lds_ld<f32x4>(lds, oxr + 1024), o2 = lds_ld<f32x4>(lds, oxr + 2048);
            const float inv = __builtin_amdgcn_rcpf(hi_half(v[8] + o2[0]));
            lds_st<u32x2>(lds, ostw, (u32x2){cvtpk((v[0] + o0[0]) * inv, (v[1] + o0[1]) * inv), cvtpk((v[2] + o0[2]) * inv, (v[3] + o0[3]) * inv)});
            lds_st<u32x2>(lds, ostw + 16, (u32x2){cvtpk((v[4] + o1[0]) * inv, (v[5] + o1[1]) * inv), cvtpk((v[6] + o1[2]) * inv, (v[7] + o1[3]) * inv)});
            if (h == 0) lds_st<u32x2>(lds, ostw + 32, (u32x2){cvtpk((v[8] + o2[0]) * inv, (v[9] + o2[1]) * inv), cvtpk((v[10] + o2[2]) * inv, (v[11] + o2[3]) * inv)});
        };
        if (kh) { send(OA); LBAR(); finish(OB); } else { send(OB); LBAR(); finish(OA); }
    }
    LBAR();
    {
        const u32x4 v0 = lds_ld<u32x4>(lds, ocl0);
        if (tid < 128) {
            const u32x4 v1 = lds_ld<u32x4>(lds, ocl1);
            __builtin_amdgcn_raw_buffer_store_b128(v1, wsr, ocg1, 0, 16);
        }
        __builtin_amdgcn_raw_buffer_store_b128(v0, wsr, ocg0, 0, 16);
    }
    WAIT_BAR(0);
    if (tid == 0) set_flag(p.ws + WS_SYNC + FLAG_O + (hp * 16 + t) * FSTR, tag64);

    int tid_ = threadIdx.x; asm volatile("" : "+v"(tid_));
    { const int tid = tid_, lane = tid & 63, r = lane & 31, h = lane >> 5;
    const bool hasD = jb < 30;
    const int dt = 5 * (xcd & 1) + jb % 5, ft = 6 * (xcd >> 1) + jb / 5;
    const __amdgpu_buffer_rsrc_t rs1 = __builtin_amdgcn_make_buffer_rsrc((void*)(p.w1 + (size_t)(256 * w) * 3072 + (hasD ? ft : 0) * 128), 0, 255 * 3072 + 128, 0x00020000);
    const __amdgpu_buffer_rsrc_t rs2 = __builtin_amdgcn_make_buffer_rsrc((void*)(p.w2 + (size_t)(192 * w) * 4096 + rt * 128), 0, 191 * 4096 + 128, 0x00020000);
    const __amdgpu_buffer_rsrc_t rs1b = __builtin_amdgcn_make_buffer_rsrc((void*)(p.w1 + (size_t)(256 * (w & 3)) * 3072 + (hasD ? ft : 0) * 128), 0, 255 * 3072 + 128, 0x00020000);
    const unsigned mlw = lds0 + w * 16384;
    u32x4 wv1; u32x2 aa1;
    {
        const int q = (lane & 15) >> 2, pp = lane & 3, g = (lane >> 4) & 1;
#pragma unroll
        for (int j = 0; j < 4; j++) wv1[j] = (lane >> 3) * 3072 + (((lane & 7) ^ (((lane >> 3) & 2) << 1)) << 4) + j * (8 * 3072 - 1024);
#pragma unroll
        for (int u = 0; u < 2; u++) aa1[u] = mlw + (8 * h + q) * 128 + (((4 * u + 2 * g + (pp >> 1)) ^ ((q & 2) << 1)) << 4) + (pp & 1) * 8;
    }
    const int kc = w >> 2, r4 = w & 3, g16 = lane >> 4, s16 = lane & 15;
    const int nown = kc ? 2 : 3, own0 = kc ? 3 : 0;
    unsigned hpk[3][2];
    {
        const int srow = 64 * rt + 16 * r4 + s16;
        u32x2 xr[3];
#pragma unroll
        for (int i = 0; i < 3; i++) xr[i] = *(const GAS u32x2*)(p.x + (size_t)srow * 1280 + cg * 160 + (own0 + (i < nown ? i : 0)) * 32 + g16 * 8);
        f32x16 c0, c1, c2, c3, c4, c5; f32x4 c6;
        const unsigned wa = lds0 + L_WO + kc * 51200 + (4 * g16 + (s16 >> 2)) * 160 + (lane & 3) * 8;
        asm volatile(C_PRE_ASM : "={v[60:75]}"(c0), "={v[76:91]}"(c1), "={v[92:107]}"(c2), "={v[108:123]}"(c3), "={v[124:139]}"(c4), "={v[140:155]}"(c5), "={v[156:159]}"(c6)
                     : "{v220}"(wa) : "memory");
        unsigned ob = WS_O + ((rt * 4 + r4) * 20 + 10 * kc) * 1024 + lane * 16;
        unsigned cxw = L_CX + w * 3072 + lane * 16, cxr = L_CX + (w ^ 4) * 3072 + lane * 16;
        unsigned stw = WS_ST + (((rt * 8 + cg) * 2 + kc) * 64 + 16 * r4 + s16) * 16, stg = WS_ST + ((rt * 16 + 4 * g16) * 64 + 16 * r4 + s16) * 16;
        unsigned htw = L_HT + (16 * r4 + s16) * 160 + own0 * 32 + g16 * 8;
        unsigned mua = L_MU + (16 * r4 + s16) * 8;
        PIN(mua);
        unsigned hl[2], hg[2];
#pragma unroll
        for (int i = 0; i < 2; i++) {
            const int u = 2 * (w + 8 * i) + (lane >> 5), ks = (u * 205) >> 10, dtt = u - ks * 5, hh2 = (lane >> 4) & 1;
            hl[i] = L_HT + (16 * ks + 8 * hh2 + (s16 >> 2)) * 160 + dtt * 32 + (lane & 3) * 8;
            hg[i] = WS_H + (((rt * 4 + ks) * 40 + cg * 5 + dtt) * 2 + hh2) * 256 + s16 * 16;
        }
        PIN(ob); PIN(cxw); PIN(cxr); PIN(stw); PIN(stg); PIN(htw); PIN(hl[0]); PIN(hg[0]); PIN(hl[1]); PIN(hg[1]);
        if (w == 0) { wait_flags(p.ws + WS_SYNC + FLAG_O + ((lane & 15) * 16 + (rt >> 1)) * FSTR, tag64); }
        LBAR();
        f32x16 q0; f32x4 q1;
        {
            unsigned st;
            asm volatile(C_MAIN_ASM : "={v[0:15]}"(q0), "={v[16:19]}"(q1), [st] "=&s"(st),
                           "+{v[60:75]}"(c0), "+{v[76:91]}"(c1), "+{v[92:107]}"(c2), "+{v[108:123]}"(c3), "+{v[124:139]}"(c4), "+{v[140:155]}"(c5), "+{v[156:159]}"(c6)
                         : "{v220}"(wa), "{v221}"(ob), [rh] "s"(wsr) : C_MAIN_CLOB, "memory", "scc");
        }
        const f32x4 acc[5] = {{q0[0], q0[1], q0[2], q0[3]}, {q0[4], q0[5], q0[6], q0[7]}, {q0[8], q0[9], q0[10], q0[11]}, {q0[12], q0[13], q0[14], q0[15]}, q1};
        if (kc == 0) { lds_st<f32x4>(lds, cxw, acc[3]); lds_st<f32x4>(lds, cxw + 1024, acc[4]); }
        else { lds_st<f32x4>(lds, cxw, acc[0]); lds_st<f32x4>(lds, cxw + 1024, acc[1]); lds_st<f32x4>(lds, cxw + 2048, acc[2]); }
        LBAR();
        float hv[3][4]; float s1 = 0.f, s2 = 0.f;
        auto fin = [&](int i, const f32x4& a, const f32x4& o) {
            hpk[i][0] = cvtpk(a[0] + o[0] + bflo(xr[i][0]), a[1] + o[1] + bfhi(xr[i][0])); hpk[i][1] = cvtpk(a[2] + o[2] + bflo(xr[i][1]), a[3] + o[3] + bfhi(xr[i][1]));
            hv[i][0] = bflo(hpk[i][0]); hv[i][1] = bfhi(hpk[i][0]); hv[i][2] = bflo(hpk[i][1]); hv[i][3] = bfhi(hpk[i][1]);
            s1 += (hv[i][0] + hv[i][1]) + (hv[i][2] + hv[i][3]);
            s2 += fmaf(hv[i][0], hv[i][0], hv[i][1] * hv[i][1]) + fmaf(hv[i][2], hv[i][2], hv[i][3] * hv[i][3]);
        };
        if (kc == 0) {
            const f32x4 o0 = lds_ld<f32x4>(lds, cxr), o1 = lds_ld<f32x4>(lds, cxr + 1024), o2 = lds_ld<f32x4>(lds, cxr + 2048);
            SBAR();
            fin(0, acc[0], o0); fin(1, acc[1], o1); fin(2, acc[2], o2);
        } else {
            const f32x4 o0 = lds_ld<f32x4>(lds, cxr), o1 = lds_ld<f32x4>(lds, cxr + 1024);
            SBAR();
            fin(0, acc[3], o0); fin(1, acc[4], o1); hpk[2][0] = 0u; hpk[2][1] = 0u; hv[2][0] = hv[2][1] = hv[2][2] = hv[2][3] = 0.f;
        }
        { auto a = __builtin_amdgcn_permlane16_swap(__float_as_uint(s1), __float_as_uint(s1), false, false); s1 = __uint_as_float(a[0]) + __uint_as_float(a[1]); }
        { auto a = __builtin_amdgcn_permlane16_swap(__float_as_uint(s2), __float_as_uint(s2), false, false); s2 = __uint_as_float(a[0]) + __uint_as_float(a[1]); }
        s1 = half_sum(s1); s2 = half_sum(s2);
        const unsigned tga = (unsigned)tag64, tgb = (unsigned)(tag64 >> 32);
        if (lane < 16) __builtin_amdgcn_raw_buffer_store_b128((u32x4){tga, __float_as_uint(s1), tgb, __float_as_uint(s2)}, wsr, stw, 0, 16);
        if (kc == 1) {
            if (hasD) {
                unsigned sv, st;
                asm volatile(D_DMA0_ASM : [sv] "=&s"(sv), [st] "=&s"(st)
                             : "{v[208:211]}"(wv1), [rs] "s"(rs1), [ml] "s"(mlw), [rs2] "s"(rs1b), [ml2] "s"(mlw - 65536), [rstep] "s"(98304u) : "memory", "scc");
            }
        } else {
            u32x4 g0, g1, g2, g3;
            __builtin_amdgcn_s_sleep(POLL_DELAY);
            for (unsigned spins = 0; spins < (1u << 20); spins++) {
                asm volatile("buffer_load_dwordx4 %0, %4, %5, 0 offen sc1\n\tbuffer_load_dwordx4 %1, %4, %5, 0 offen offset:1024 sc1\n\t"
                             "buffer_load_dwordx4 %2, %4, %5, 0 offen offset:2048 sc1\n\tbuffer_load_dwordx4 %3, %4, %5, 0 offen offset:3072 sc1\n\ts_waitcnt vmcnt(0)"
                             : "=&v"(g0), "=&v"(g1), "=&v"(g2), "=&v"(g3) : "v"(stg), "s"(wsr) : "memory");
                const bool ok = g0[0] == tga && g0[2] == tgb && g1[0] == tga && g1[2] == tgb && g2[0] == tga && g2[2] == tgb && g3[0] == tga && g3[2] == tgb;
                if (__all(ok)) break;
            }
            float a1 = (__uint_as_float(g0[1]) + __uint_as_float(g1[1])) + (__uint_as_float(g2[1]) + __uint_as_float(g3[1]));
            float a2 = (__uint_as_float(g0[3]) + __uint_as_float(g1[3])) + (__uint_as_float(g2[3]) + __uint_as_float(g3[3]));
            { auto a = __builtin_amdgcn_permlane16_swap(__float_as_uint(a1), __float_as_uint(a1), false, false); a1 = __uint_as_float(a[0]) + __uint_as_float(a[1]); }
            { auto a = __builtin_amdgcn_permlane16_swap(__float_as_uint(a2), __float_as_uint(a2), false, false); a2 = __uint_as_float(a[0]) + __uint_as_float(a[1]); }
            a1 = half_sum(a1); a2 = half_sum(a2);
            const float m_ = a1 * (1.f / DM);
            const float var = fmaxf(a2 * (1.f / DM) - m_ * m_, 0.f);
            const float r1 = __builtin_amdgcn_rsqf(var + LN_EPS);
            if (lane < 16) lds_st<f32x2>(lds, mua, (f32x2){m_, r1 * __builtin_amdgcn_rsqf(var * r1 * r1 + LN_EPS)});
        }
        LBAR();
        const f32x2 mr = lds_ld<f32x2>(lds, mua);
        const float mu = mr[0], rho = mr[1];
#pragma unroll
        for (int i = 0; i < 3; i++)
            if (i < nown) lds_st<u32x2>(lds, htw + i * 32, (u32x2){cvtpk((hv[i][0] - mu) * rho, (hv[i][1] - mu) * rho), cvtpk((hv[i][2] - mu) * rho, (hv[i][3] - mu) * rho)});
        LBAR();
        {
            const bf16x8 v = cat44(lds_tr(lds, hl[0]), lds_tr(lds, hl[0] + 640));
            if (w < 2) {
                const bf16x8 v2 = cat44(lds_tr(lds, hl[1]), lds_tr(lds, hl[1] + 640));
                __builtin_amdgcn_raw_buffer_store_b128(__builtin_bit_cast(u32x4, v2), wsr, hg[1], 0, 16);
            }
            __builtin_amdgcn_raw_buffer_store_b128(__builtin_bit_cast(u32x4, v), wsr, hg[0], 0, 16);
        }
        WAIT_BAR(0);
        if (tid == 0) set_flag(p.ws + WS_SYNC + FLAG_H + (rt * 8 + cg) * FSTR, tag64);
    }
    u32x4 wv2, ba2;
    {
        const int q = (lane & 15) >> 2, pp = lane & 3;
        const int row = 16 * (g16 >> 1) + 4 * (g16 & 1) + q;
#pragma unroll
        for (int j = 0; j < 4; j++) {
            wv2[j] = (lane >> 3) * 4096 + (((lane & 7) ^ ((((lane >> 3) >> 1) & 3) << 1)) << 4) + j * (8 * 4096 - 1024);
            ba2[j] = mlw + row * 128 + ((j ^ ((row >> 1) & 3)) << 5) + pp * 8;
        }
    }
    if (hasD) {
        f32x16 ah0, ah1, ah2, ah3;
        asm volatile(D_PRE_ASM : "={v[64:79]}"(ah0), "={v[80:95]}"(ah1), "={v[96:111]}"(ah2), "={v[112:127]}"(ah3) : "{v[212:213]}"(aa1) : "memory");
        {
            unsigned sv, st;
            asm volatile(D_DMA1_ASM : [sv] "=&s"(sv), [st] "=&s"(st) : "{v[208:211]}"(wv1), [rs] "s"(rs1), [ml] "s"(mlw), [rstep] "s"(98304u) : "memory", "scc");
        }
        const int uo = w >> 2, vo = (w >> 1) & 1, ho = w & 1;
        const int f0 = 64 * ft + 32 * uo + 16 * ho + 4 * h;
        const u32x2 bA = *(const GAS u32x2*)(p.b1 + f0 * 2), bB = *(const GAS u32x2*)(p.b1 + (f0 + 8) * 2);
        unsigned hb = WS_H + ((16 * w) * 40 + 4 * dt + (r >> 4)) * 512 + h * 256 + (r & 15) * 16;
        unsigned uw = WS_U + (((2 * ft + uo) * 40 + 4 * dt + 2 * vo + (r >> 4)) * 4 + 2 * ho + h) * 256 + (r & 15) * 16;
        PIN(hb); PIN(uw);
        if (w == 0) {
            const int c0 = (64 * dt) / 80, c1 = (64 * dt + 63) / 80;
            wait_flags(p.ws + WS_SYNC + FLAG_H + ((lane >> 1) * 8 + ((lane & 1) ? c1 : c0)) * FSTR, tag64);
            if (b == 0 && lane == 0) set_flag(p.ws + WS_EPOCH, tag64);
        }
        LBAR();
        f32x16 a0, a1, a2, a3;
        {
            unsigned st;
            asm volatile(D_MAIN_ASM : "={v[0:15]}"(a0), "={v[16:31]}"(a1), "={v[32:47]}"(a2), "={v[48:63]}"(a3), [st] "=&s"(st)
                         : "{v[64:79]}"(ah0), "{v[80:95]}"(ah1), "{v[96:111]}"(ah2), "{v[112:127]}"(ah3), "{v[212:213]}"(aa1), "{v214}"(hb), [rh] "s"(wsr)
                         : D_MAIN_CLOB, "memory", "scc");
        }
        f32x4 ownA = {}, ownB = {};
        const unsigned dw_ = w * 16384 + lane * 16;
#define D_UNIT(acc, dest, o) do { const f32x4 x0 = {acc[o], acc[o + 1], acc[o + 2], acc[o + 3]}, x1 = {acc[o + 4], acc[o + 5], acc[o + 6], acc[o + 7]}; \
            if ((dest) != w) { lds_st<f32x4>(lds, dw_ + (dest) * 2048, x0); lds_st<f32x4>(lds, dw_ + (dest) * 2048 + 1024, x1); } \
            else { ownA = x0; ownB = x1; } } while (0)
        D_UNIT(a0, 0, 0); D_UNIT(a0, 1, 8); D_UNIT(a1, 2, 0); D_UNIT(a1, 3, 8); D_UNIT(a2, 4, 0); D_UNIT(a2, 5, 8); D_UNIT(a3, 6, 0); D_UNIT(a3, 7, 8);
        LBAR();
        {
            f32x4 ta_[7], tb_[7];
#pragma unroll
            for (int i = 1; i < 8; i++) {
                const unsigned a = ((w + i) & 7) * 16384 + w * 2048 + lane * 16;
                ta_[i - 1] = lds_ld<f32x4>(lds, a); tb_[i - 1] = lds_ld<f32x4>(lds, a + 1024);
            }
            SBAR();
            ownA = ((ownA + ta_[0]) + (ta_[1] + ta_[2])) + ((ta_[3] + ta_[4]) + (ta_[5] + ta_[6]));
            ownB = ((ownB + tb_[0]) + (tb_[1] + tb_[2])) + ((tb_[3] + tb_[4]) + (tb_[5] + tb_[6]));
        }
        LBAR();
        {
            unsigned sv, st;
            asm volatile(E_DMA0_ASM : [sv] "=&s"(sv), [st] "=&s"(st) : "{v[224:227]}"(wv2), [rs] "s"(rs2), [ml] "s"(mlw), [rstep] "s"(131072u) : "memory", "scc");
        }
        const float own[8] = {ownA[0], ownA[1], ownA[2], ownA[3], ownB[0], ownB[1], ownB[2], ownB[3]};
        const float bias[8] = {bflo(bA[0]), bfhi(bA[0]), bflo(bA[1]), bfhi(bA[1]), bflo(bB[0]), bfhi(bB[0]), bflo(bB[1]), bfhi(bB[1])};
        float ge[8];
#pragma unroll
        for (int e = 0; e < 8; e++) {
            const float x = own[e] + bias[e];
            const float z = x * fmaf(x * x, -0.044715f * 2.302208198f, -2.302208198f);
            ge[e] = x * __builtin_amdgcn_rcpf(1.f + __builtin_amdgcn_exp2f(z));
        }
        __builtin_amdgcn_raw_buffer_store_b128((u32x4){cvtpk(ge[0], ge[1]), cvtpk(ge[2], ge[3]), cvtpk(ge[4], ge[5]), cvtpk(ge[6], ge[7])}, wsr, uw, 0, 16);
        WAIT_BAR(0);
        if (tid == 0) set_flag(p.ws + WS_SYNC + FLAG_U + (ft * 10 + dt) * FSTR, tag64);
    } else {
        unsigned sv, st;
        asm volatile(E_DMA0_ASM : [sv] "=&s"(sv), [st] "=&s"(st) : "{v[224:227]}"(wv2), [rs] "s"(rs2), [ml] "s"(mlw), [rstep] "s"(131072u) : "memory", "scc");
    }

    {
        const __amdgpu_buffer_rsrc_t rso = __builtin_amdgcn_make_buffer_rsrc(p.out, 0, SEQ * 1280, 0x00020000);
        f32x16 bp0, bp1;
        asm volatile(E_PRE_ASM : "={v[160:175]}"(bp0), "={v[176:191]}"(bp1) : "{v[228:231]}"(ba2) : "memory");
        {
            unsigned sv, st;
            asm volatile(E_DMA1_ASM : [sv] "=&s"(sv), [st] "=&s"(st) : "{v[224:227]}"(wv2), [rs] "s"(rs2), [ml] "s"(mlw), [rstep] "s"(131072u) : "memory", "scc");
        }
        const float b2v = bflo((unsigned)*(const GAS unsigned short*)(p.b2 + (64 * rt + 16 * r4 + s16) * 2));
        unsigned ub = WS_U + ((6 * w) * 40 + 5 * cg) * 1024 + lane * 16;
        unsigned oa = (64 * rt + 16 * r4 + s16) * 1280 + cg * 160 + own0 * 32 + g16 * 8;
        PIN(ub); PIN(oa);
        if (w == 0) {
            const int c0 = (80 * cg) / 64, c1 = (80 * cg + 79) / 64, f_ = min(lane >> 1, 23);
            wait_flags(p.ws + WS_SYNC + FLAG_U + (f_ * 10 + ((lane & 1) ? c1 : c0)) * FSTR, tag64);
        }
        LBAR();
        f32x16 e0, e1, e2, e3, e4;
        {
            unsigned st;
            asm volatile(E_MAIN_ASM : "={v[0:15]}"(e0), "={v[16:31]}"(e1), "={v[32:47]}"(e2), "={v[48:63]}"(e3), "={v[64:79]}"(e4), [st] "=&s"(st)
                         : "{v[160:175]}"(bp0), "{v[176:191]}"(bp1), "{v[228:231]}"(ba2), "{v232}"(ub), [rh] "s"(wsr)
                         : E_MAIN_CLOB, "memory", "scc");
        }
        f32x4 own[3] = {{}, {}, {}};
        const unsigned ew0 = w * 16384 + lane * 16, ew1 = 131072 - 16384 + w * 4096 + lane * 16;
#define E_TILE(acc, o, st, d) do { const f32x4 x0 = {acc[o], acc[o + 1], acc[o + 2], acc[o + 3]}; constexpr int dest = ((d) >= 3 ? 4 : 0) + (st), ti = (st) * 5 + (d); \
            if (dest != w) lds_st<f32x4>(lds, (ti < 16 ? ew0 : ew1) + ti * 1024, x0); else own[(d) >= 3 ? (d) - 3 : (d)] = x0; } while (0)
        E_TILE(e0, 0, 0, 0); E_TILE(e0, 4, 0, 1); E_TILE(e0, 8, 0, 2); E_TILE(e0, 12, 0, 3);
        E_TILE(e1, 0, 0, 4); E_TILE(e1, 4, 1, 0); E_TILE(e1, 8, 1, 1); E_TILE(e1, 12, 1, 2);
        E_TILE(e2, 0, 1, 3); E_TILE(e2, 4, 1, 4); E_TILE(e2, 8, 2, 0); E_TILE(e2, 12, 2, 1);
        E_TILE(e3, 0, 2, 2); E_TILE(e3, 4, 2, 3); E_TILE(e3, 8, 2, 4); E_TILE(e3, 12, 3, 0);
        E_TILE(e4, 0, 3, 1); E_TILE(e4, 4, 3, 2); E_TILE(e4, 8, 3, 3); E_TILE(e4, 12, 3, 4);
        LBAR();
        auto finish = [&](auto NOWN) {
            constexpr int n = decltype(NOWN)::value;
            f32x4 t[n][7];
#pragma unroll
            for (int i = 0; i < n; i++) {
                const int ti = r4 * 5 + own0 + i;
                const unsigned rb = (ti < 16 ? ti * 1024 : 131072 + (ti - 16) * 1024) + lane * 16, rs_ = ti < 16 ? 16384 : 4096;
#pragma unroll
                for (int sw = 1; sw < 8; sw++) t[i][sw - 1] = lds_ld<f32x4>(lds, rb + ((w + sw) & 7) * rs_);
            }
            SBAR();
#pragma unroll
            for (int i = 0; i < n; i++) {
                const f32x4 sm = ((own[i] + t[i][0]) + (t[i][1] + t[i][2])) + ((t[i][3] + t[i][4]) + (t[i][5] + t[i][6]));
                const float o0 = sm[0] + b2v + bflo(hpk[i][0]), o1 = sm[1] + b2v + bfhi(hpk[i][0]), o2 = sm[2] + b2v + bflo(hpk[i][1]), o3 = sm[3] + b2v + bfhi(hpk[i][1]);
                __builtin_amdgcn_raw_buffer_store_b64((u32x2){cvtpk(o0, o1), cvtpk(o2, o3)}, rso, oa + i * 32, 0, 0);
            }
        };
        if (kc == 0) finish(std::integral_constant<int, 3>{}); else finish(std::integral_constant<int, 2>{});
    }
    }
}

extern "C" void kernel_launch(void* const* d_in, const int* in_sizes, int n_in, void* d_out, int out_size, void* d_ws, size_t ws_size, hipStream_t stream) {
    Params p;
    p.x = (const char*)d_in[0]; p.wqkv = (const char*)d_in[1]; p.wo = (const char*)d_in[2];
    p.w1 = (const char*)d_in[4]; p.b1 = (const char*)d_in[5]; p.w2 = (const char*)d_in[6]; p.b2 = (const char*)d_in[7];
    p.out = (char*)d_out; p.ws = (char*)d_ws;
    hipFuncSetAttribute((const void*)mega, hipFuncAttributeMaxDynamicSharedMemorySize, LDS_BYTES);
    mega<<<256, 512, LDS_BYTES, stream>>>(p);
}
```
